```python
import torch

def _softmax(x, axis=-1):
    return torch.softmax(x, dim=axis)
def _gelu(x):
    return torch.nn.functional.gelu(x, approximate="tanh")
def _silu(x):
    return torch.nn.functional.silu(x)
def _sigmoid(x):
    return torch.sigmoid(x)
def _layernorm(x, axis=-1, eps=1e-5):
    m = x.mean(dim=axis, keepdim=True)
    v = x.var(dim=axis, keepdim=True, unbiased=False)
    return (x - m) / torch.sqrt(v + eps)
def _scatter_add(tgt, idx, src, axis=0):
    return tgt.clone().index_add_(axis, idx, src)
def _segment_sum(src, seg, n):
    out = torch.zeros((n,) + src.shape[1:], dtype=src.dtype)
    return out.index_add_(0, seg, src)
def _pad(x, pw):
    flat = tuple(v for pair in reversed(pw) for v in pair)
    return torch.nn.functional.pad(x, flat)

kernel_name = 'recurrent_scan__linear_attn_block__glu_gated_reglu__prompt'

def setup_inputs():

    _w = (lambda a: (lambda t: t.to(torch.bfloat16) if t.is_floating_point() else t)(torch.as_tensor(a)))
    def _A_setup():
        B, S, D = 1, 2048, 640
        return {
            "x": _w((lambda *s: torch.randn(*s, dtype=torch.bfloat16) * 0.05)(B, S, D)),
            "wqkv": _w((lambda *s: torch.randn(*s, dtype=torch.bfloat16) * 0.8)(D, 3 * D)),
            "wo": _w((lambda *s: torch.randn(*s, dtype=torch.bfloat16) * 0.8)(D, D)),
        }
    _A_in = _A_setup()

    def _B_setup():
        B, S, D, F = 1, 2048, 640, 1728
        return {
            "x": _w((lambda *s: torch.randn(*s, dtype=torch.bfloat16) * 0.05)(B, S, D)),
            "W_gate": _w((lambda *s: torch.randn(*s, dtype=torch.bfloat16) * 0.2)(D, F) * (2.0 / D) ** 0.5),
            "W_up": _w((lambda *s: torch.randn(*s, dtype=torch.bfloat16) * 0.2)(D, F) * (2.0 / D) ** 0.5),
            "W_down": _w((lambda *s: torch.randn(*s, dtype=torch.bfloat16) * 0.2)(F, D) * (1.0 / F) ** 0.5),
        }
    _B_in = _B_setup()

    return {
        "A_x": _A_in["x"],
        "A_wqkv": _A_in["wqkv"],
        "A_wo": _A_in["wo"],
        "B_x": _B_in["x"],
        "B_W_gate": _B_in["W_gate"],
        "B_W_up": _B_in["W_up"],
        "B_W_down": _B_in["W_down"],
    }

def reference(A_x, A_wqkv, A_wo, B_x, B_W_gate, B_W_up, B_W_down):
    def _A_ref(x, wqkv, wo):
        B, S, D = x.shape
        H = 32; Dh = D // H
        qkv = (lambda x, *d: x.permute(*d))((x @ wqkv).reshape(B, S, 3, H, Dh), 2, 0, 3, 1, 4)
        q, k, v = qkv[0], qkv[1], qkv[2]
        phi = lambda t: torch.where(t > 0, t + 1, torch.exp(t)) + 1e-6
        pq, pk = phi(q), phi(k)
        kv = torch.einsum("bhsd,bhse->bhde", pk, v)
        z = torch.einsum("bhsd->bhd", pk)
        num = torch.einsum("bhqd,bhde->bhqe", pq, kv)
        den = torch.einsum("bhqd,bhd->bhq", pq, z)[..., None] + 1e-6
        o = num / den
        return (lambda x, *d: x.permute(*d))(o, 0, 2, 1, 3).reshape(B, S, D) @ wo

    def _B_ref(x, W_gate, W_up, W_down):
        gate = torch.einsum("bsd,df->bsf", x, W_gate)
        up = torch.einsum("bsd,df->bsf", x, W_up)
        gate = gate * (lambda x, dt: x.to(dt))(gate > 0, gate.dtype)
        h = gate * up
        return torch.einsum("bsf,fd->bsd", h, W_down)

    x = A_x
    xn = _layernorm(x, axis=-1)
    s = _A_ref(xn, A_wqkv, A_wo)
    g = _silu(_B_ref(xn, B_W_gate, B_W_up, B_W_down))
    return x + s * g
```

```cpp
#include <hip/hip_runtime.h>
#include <hip/hip_bf16.h>
typedef unsigned short u16;
__device__ __forceinline__ float b2f(u16 v){ return __uint_as_float(((unsigned)v)<<16); }
__device__ __forceinline__ u16 f2b(float f){ unsigned u=__float_as_uint(f); u += 0x7fffu + ((u>>16)&1u); return (u16)(u>>16); }
__device__ __forceinline__ float rb(float f){ return b2f(f2b(f)); }
constexpr int S=2048, D=640, F=1728, H=32, DH=20;

__global__ void ln_k(const u16* x, u16* xn){
  int row=blockIdx.x; int t=threadIdx.x;
  float s=0; for(int i=t;i<D;i+=64) s+=b2f(x[row*D+i]);
  for(int o=32;o;o>>=1) s+=__shfl_xor(s,o);
  float m=s/D; float q=0;
  for(int i=t;i<D;i+=64){ float d=b2f(x[row*D+i])-m; q+=d*d; }
  for(int o=32;o;o>>=1) q+=__shfl_xor(q,o);
  float mb=rb(m), vb=rb(q/D); float sd=rb(sqrtf(rb(vb+1e-5f)));
  for(int i=t;i<D;i+=64) xn[row*D+i]=f2b(rb(b2f(x[row*D+i])-mb)/sd);
}
__global__ void gemm_k(const u16* A,const u16* B,u16* C,int M,int N,int K){
  __shared__ float As[16][17], Bs[16][17];
  int r=blockIdx.y*16+threadIdx.y, c=blockIdx.x*16+threadIdx.x; float acc=0;
  for(int k0=0;k0<K;k0+=16){
    As[threadIdx.y][threadIdx.x]=b2f(A[r*K+k0+threadIdx.x]);
    Bs[threadIdx.y][threadIdx.x]=b2f(B[(k0+threadIdx.y)*N+c]);
    __syncthreads();
    for(int k=0;k<16;k++) acc+=As[threadIdx.y][k]*Bs[k][threadIdx.x];
    __syncthreads();
  }
  C[r*N+c]=f2b(acc);
}
__device__ __forceinline__ float phi(float t){ float r = t>0 ? rb(t+1.f) : rb(__expf(t)); return rb(r+1e-6f); }
__global__ void kv_k(const u16* qkv, u16* kv, u16* z){
  int h=blockIdx.x; int t=threadIdx.x;
  if(t<400){ int d=t/20,e=t%20; float a=0; for(int s=0;s<S;s++) a+=phi(b2f(qkv[s*1920+640+h*20+d]))*b2f(qkv[s*1920+1280+h*20+e]); kv[h*400+t]=f2b(a);}
  else if(t<420){ int d=t-400; float a=0; for(int s=0;s<S;s++) a+=phi(b2f(qkv[s*1920+640+h*20+d])); z[h*20+d]=f2b(a);}
}
__global__ void o_k(const u16* qkv,const u16* kv,const u16* z,u16* o){
  int s=blockIdx.x; int t=threadIdx.x;
  int h=t/20,e=t%20; float n=0,dn=0;
  for(int d=0;d<20;d++){ float p=phi(b2f(qkv[s*1920+h*20+d])); n+=p*b2f(kv[h*400+d*20+e]); dn+=p*b2f(z[h*20+d]); }
  o[s*640+t]=f2b(rb(n)/rb(rb(dn)+1e-6f));
}
__global__ void h_k(const u16* g,const u16* u,u16* h,int n){ int i=blockIdx.x*256+threadIdx.x; if(i<n){ float a=b2f(g[i]); a=a>0?a:0.f; h[i]=f2b(a*b2f(u[i])); } }
__global__ void fin_k(const u16* x,const u16* s,const u16* b,u16* out,int n){ int i=blockIdx.x*256+threadIdx.x; if(i<n){ float v=b2f(b[i]); float g=rb(v/(1.f+__expf(-v))); out[i]=f2b(b2f(x[i])+rb(b2f(s[i])*g)); } }

extern "C" void kernel_launch(void* const* d_in, const int* in_sizes, int n_in, void* d_out, int out_size, void* d_ws, size_t ws_size, hipStream_t stream){
  const u16* x=(const u16*)d_in[0]; const u16* wqkv=(const u16*)d_in[1]; const u16* wo=(const u16*)d_in[2];
  const u16* wg=(const u16*)d_in[4]; const u16* wu=(const u16*)d_in[5]; const u16* wd=(const u16*)d_in[6];
  u16* ws=(u16*)d_ws; u16* xn=ws; u16* qkv=xn+S*D; u16* g=qkv+S*1920; u16* u=g+S*F; u16* hh=u+S*F; u16* kv=hh+S*F; u16* z=kv+16384; u16* o=z+1024; u16* sb=o+S*D; u16* bb=sb+S*D;
  ln_k<<<S,64,0,stream>>>(x,xn);
  gemm_k<<<dim3(1920/16,S/16),dim3(16,16),0,stream>>>(xn,wqkv,qkv,S,1920,D);
  gemm_k<<<dim3(F/16,S/16),dim3(16,16),0,stream>>>(xn,wg,g,S,F,D);
  gemm_k<<<dim3(F/16,S/16),dim3(16,16),0,stream>>>(xn,wu,u,S,F,D);
  kv_k<<<H,448,0,stream>>>(qkv,kv,z);
  o_k<<<S,640,0,stream>>>(qkv,kv,z,o);
  h_k<<<(S*F+255)/256,256,0,stream>>>(g,u,hh,S*F);
  gemm_k<<<dim3(D/16,S/16),dim3(16,16),0,stream>>>(o,wo,sb,S,D,D);
  gemm_k<<<dim3(D/16,S/16),dim3(16,16),0,stream>>>(hh,wd,bb,S,D,F);
  fin_k<<<(S*D+255)/256,256,0,stream>>>(x,sb,bb,(u16*)d_out,S*D);
}
```

```cpp
#include <hip/hip_runtime.h>
typedef unsigned short u16;
typedef unsigned int u32;
typedef short s16x4 __attribute__((ext_vector_type(4)));
typedef short s16x8 __attribute__((ext_vector_type(8)));
typedef __bf16 bf16x8 __attribute__((ext_vector_type(8)));
typedef __bf16 bf16x2 __attribute__((ext_vector_type(2)));
typedef float f32x2 __attribute__((ext_vector_type(2)));
typedef float f32x4 __attribute__((ext_vector_type(4)));
typedef float f32x16 __attribute__((ext_vector_type(16)));
typedef u32 u32x2 __attribute__((ext_vector_type(2)));
typedef u32 u32x4 __attribute__((ext_vector_type(4)));
#define LAS __attribute__((address_space(3)))
#define GAS __attribute__((address_space(1)))
#define DI __device__ __forceinline__

constexpr int S_ = 2048, D_ = 640, F_ = 1728;
constexpr size_t WS_FLAG = 0;
constexpr size_t WS_KVP  = 32768;
constexpr size_t WS_HF   = 524288;
constexpr size_t WS_QF   = WS_HF + 32 * 54 * 4096;
constexpr size_t WS_OF   = WS_QF + 128 * 20 * 1024;
constexpr size_t WS_WDF  = WS_OF + 128 * 20 * 1024;
constexpr size_t WS_WOF  = WS_WDF + 8 * 54 * 5120;
constexpr size_t WS_XN   = WS_WOF + 8 * 20 * 5120;
constexpr size_t WS_END  = WS_XN + S_ * D_ * 2;
constexpr size_t WS_DBG  = 64u << 20;

#define K1_RB 4

DI u32 pk2(float a, float b) { f32x2 v = {a, b}; return __builtin_bit_cast(u32, __builtin_convertvector(v, bf16x2)); }
DI float blo(u32 d) { return __uint_as_float(d << 16); }
DI float bhi(u32 d) { return __uint_as_float(d & 0xffff0000u); }
DI float rbf(float f) { return blo(pk2(f, f)); }
DI u32x4 mk_srd(const void* p, u32 bytes) {
  unsigned long long a = (unsigned long long)p;
  u32x4 r;
  r[0] = __builtin_amdgcn_readfirstlane((u32)a); r[1] = __builtin_amdgcn_readfirstlane((u32)(a >> 32) & 0xffffu);
  r[2] = __builtin_amdgcn_readfirstlane(bytes); r[3] = 0x00020000u;
  return r;
}
#define DMA16(m0v, voff, srd, soff) \
  asm volatile("s_mov_b32 m0, %0\n\ts_nop 0\n\tbuffer_load_dwordx4 %1, %2, %3 offen lds" ::"s"(m0v), "v"(voff), "s"(srd), "s"(soff) : "memory")
#define WAIT_V(n) asm volatile("s_waitcnt vmcnt(" #n ")" ::: "memory")
#define WAIT_L0() asm volatile("s_waitcnt lgkmcnt(0)" ::: "memory")
#define BAR() __builtin_amdgcn_s_barrier()
#define SCHED() __builtin_amdgcn_sched_barrier(0)
#define MFMA32(a, b, c) __builtin_amdgcn_mfma_f32_32x32x16_bf16(__builtin_bit_cast(bf16x8, a), __builtin_bit_cast(bf16x8, b), c, 0, 0, 0)
#define MFMA16(a, b, c) __builtin_amdgcn_mfma_f32_16x16x32_bf16(__builtin_bit_cast(bf16x8, a), __builtin_bit_cast(bf16x8, b), c, 0, 0, 0)

#define STAMP_INIT() do {} while (0)
#define STAMP(ws, k, i) do {} while (0)
#define STAMP_FLUSH(ws, k) do {} while (0)
#define SP_ARG
#define SP_PASS

__global__ __launch_bounds__(128) void k0_ln(const u16* __restrict__ x, char* __restrict__ ws) {
  STAMP_INIT(); STAMP(ws, 0, 0);
  const int lane = threadIdx.x & 15, row = blockIdx.x * 8 + (threadIdx.x >> 4);
  const u32x4* src = (const u32x4*)(x + row * D_);
  u32x4 v[5];
#pragma unroll
  for (int i = 0; i < 5; i++) v[i] = src[lane + 16 * i];
  float s = 0;
#pragma unroll
  for (int i = 0; i < 5; i++)
#pragma unroll
    for (int j = 0; j < 4; j++) s += blo(v[i][j]) + bhi(v[i][j]);
#pragma unroll
  for (int o = 8; o; o >>= 1) s += __shfl_xor(s, o);
  const float m = s * (1.f / D_);
  float q = 0;
#pragma unroll
  for (int i = 0; i < 5; i++)
#pragma unroll
    for (int j = 0; j < 4; j++) { float a = blo(v[i][j]) - m, b = bhi(v[i][j]) - m; q += a * a + b * b; }
#pragma unroll
  for (int o = 8; o; o >>= 1) q += __shfl_xor(q, o);
  const float mb = rbf(m), sd = rbf(sqrtf(rbf(rbf(q * (1.f / D_)) + 1e-5f))), rs = 1.f / sd;
  u32x4* dst = (u32x4*)(ws + WS_XN) + row * 80;
#pragma unroll
  for (int i = 0; i < 5; i++) {
    u32x4 o;
#pragma unroll
    for (int j = 0; j < 4; j++) o[j] = pk2(rbf(blo(v[i][j]) - mb) * rs, rbf(bhi(v[i][j]) - mb) * rs);
    dst[lane + 16 * i] = o;
  }
  STAMP(ws, 0, 1); STAMP_FLUSH(ws, 0);
}

constexpr int XS = 32768;
constexpr int W_IMG1 = 12288 + 32;
constexpr int WSTG = 24576 + 64;
constexpr int LDS_W = 2 * XS;
constexpr int K1_LDS = LDS_W + 3 * WSTG;

DI float phi(float q) {
  float e = __builtin_amdgcn_exp2f(q * 1.44269504089f);
  e = fminf(e, 1.f);
  return fmaxf(q, 0.f) + 1e-6f + e;
}

template <int TYPE>
DI void k1_body(char* smem, const u16* xa, const u16* w0, const u16* w1, u32 wbytes0, u32 wbytes1, int pitch, int rb, int cb, char* ws SP_ARG) {
  constexpr int MT = TYPE == 0 ? 2 : 1, NT = TYPE == 0 ? 3 : 5;
  const int lane = threadIdx.x & 63;
  const int wave = __builtin_amdgcn_readfirstlane(threadIdx.x >> 6);
  const int grp = wave >> 2, wg = wave & 3;
  const u32 lds0 = (u32)(size_t)(LAS char*)smem;
  const u32x4 srdx = mk_srd(xa + (size_t)rb * 256 * D_, 256 * D_ * 2);
  const u32x4 srdw = grp ? mk_srd(w1, wbytes1) : mk_srd(w0, wbytes0);
  const int xw = (wave + 4) & 7;
  u32 vx[2], vw[3];
#pragma unroll
  for (int i = 0; i < 2; i++) { int r = 8 * i + (lane >> 3); vx[i] = (32 * xw + r) * (D_ * 2) + (((lane & 7) ^ ((r >> 1) & 7)) << 4); }
#pragma unroll
  for (int i = 0; i < 3; i++) { int o = (3 * wg + i) * 1024 + lane * 16; vw[i] = (o / 192) * pitch + (o % 192); }
  const u32 mx = lds0 + xw * 4096, mw = lds0 + LDS_W + grp * W_IMG1 + wg * 3072;
  auto issue_x = [&](int t) {
    const u32 m = mx + (t & 1) * XS, so = t * 128;
    DMA16(m, vx[0], srdx, so); DMA16(m + 1024, vx[1], srdx, so);
    DMA16(m + 2048, vx[0], srdx, so + 16 * D_ * 2); DMA16(m + 3072, vx[1], srdx, so + 16 * D_ * 2);
  };
  auto issue_w = [&](int t, int slot) {
    const u32 m = mw + slot * WSTG, so = t * 64 * pitch;
    DMA16(m, vw[0], srdw, so); DMA16(m + 1024, vw[1], srdw, so); DMA16(m + 2048, vw[2], srdw, so);
  };
  asm volatile("s_nop 4" ::: "memory");
  issue_x(0); issue_w(0, 0);
  if (grp) { issue_x(1); issue_w(1, 1); } else { issue_w(1, 1); issue_x(1); }
  issue_w(2, 2);
  const int hh = lane >> 5, r31 = lane & 31;
  const int row0 = TYPE == 0 ? (wave >> 1) * 64 : wave * 32;
  u32 ax[4];
#pragma unroll
  for (int ks = 0; ks < 4; ks++) ax[ks] = (row0 + r31) * 128 + (((2 * ks + hh) ^ ((r31 >> 1) & 7)) << 4);
  const int b16 = (lane >> 4) & 1;
  const u32 awc = LDS_W + (8 * hh + ((lane & 15) >> 2)) * 192 + (lane & 3) * 8;
  u32 aw0, aw1 = 0;
  if (TYPE == 0) aw0 = awc + b16 * W_IMG1 + (wave & 1) * 96;
  else if (TYPE == 1) { aw0 = awc + b16 * 32 + (cb & 1) * 32; aw1 = awc + (cb & 1) * 32 + (b16 ? W_IMG1 : 128); }
  else aw0 = awc + b16 * 32;

  f32x16 acc[MT][NT];
#pragma unroll
  for (int m = 0; m < MT; m++)
#pragma unroll
    for (int n = 0; n < NT; n++)
#pragma unroll
      for (int i = 0; i < 16; i++) acc[m][n][i] = 0.f;
  s16x8 xf[4][MT], wf[4][NT];

  auto trd = [&](u32 a) { return __builtin_amdgcn_ds_read_tr16_b64_v4i16((LAS s16x4*)(smem + a)); };
  auto load_frags = [&](int xslot, int wslot) {
    const u32 xo = xslot * XS, wo = wslot * WSTG;
#pragma unroll
    for (int ks = 0; ks < 4; ks++) {
#pragma unroll
      for (int m = 0; m < MT; m++) xf[ks][m] = *(LAS s16x8*)(smem + ax[ks] + xo + m * 4096);
#pragma unroll
      for (int n = 0; n < NT; n++) {
        u32 a;
        if (TYPE == 0) a = aw0 + n * 32;
        else if (TYPE == 1) a = n < 2 ? aw0 + n * 64 : (n == 2 ? aw1 : aw0 + W_IMG1 + 32 + (n - 3) * 64);
        else a = n < 3 ? aw0 + n * 64 : aw0 + W_IMG1 + (n - 3) * 64;
        a += wo + ks * 16 * 192;
        s16x4 lo = trd(a), hi = trd(a + 4 * 192);
        wf[ks][n] = __builtin_shufflevector(lo, hi, 0, 1, 2, 3, 4, 5, 6, 7);
      }
    }
  };
  auto compute = [&]() {
    __builtin_amdgcn_s_setprio(1);
#pragma unroll
    for (int ks = 0; ks < 4; ks++)
#pragma unroll
      for (int m = 0; m < MT; m++)
#pragma unroll
        for (int n = 0; n < NT; n++)
          acc[m][n] = TYPE == 1 ? MFMA32(xf[ks][m], wf[ks][n], acc[m][n]) : MFMA32(wf[ks][n], xf[ks][m], acc[m][n]);
    __builtin_amdgcn_s_setprio(0);
  };

  WAIT_V(10); BAR();
  STAMP(ws, 1, 1);
  if (grp) BAR();
  SCHED();
  load_frags(0, 0);
  if (grp) issue_x(2);
  WAIT_L0(); WAIT_V(7); SCHED(); BAR(); SCHED();
  compute(); SCHED(); BAR(); SCHED();
  int wslot = 1;
#pragma unroll 1
  for (int t = 1; t < 8; t++) {
    load_frags(t & 1, wslot);
    issue_x(t + 1 + grp);
    issue_w(t + 2, wslot == 0 ? 2 : wslot - 1);
    wslot = wslot == 2 ? 0 : wslot + 1;
    WAIT_L0(); WAIT_V(7); SCHED(); BAR(); SCHED();
    compute(); SCHED(); BAR(); SCHED();
  }
  load_frags(0, 2);
  if (!grp) { issue_x(9); WAIT_V(4); } else { WAIT_V(0); }
  WAIT_L0(); SCHED(); BAR(); SCHED();
  compute(); SCHED(); BAR(); SCHED();
  load_frags(1, 0);
  WAIT_L0(); WAIT_V(0); SCHED(); BAR(); SCHED();
  compute(); SCHED();
  if (!grp) BAR();
  SCHED();
  STAMP(ws, 1, 2);

  if (TYPE == 0) {
#pragma unroll
    for (int m = 0; m < MT; m++) {
      const int tok = rb * 256 + row0 + m * 32 + r31;
#pragma unroll
      for (int n = 0; n < NT; n++) {
        const int f16 = cb * 6 + (wave & 1) * 3 + n;
        u32x4 o;
#pragma unroll
        for (int j = 0; j < 4; j++)
          o[j] = pk2(fmaxf(acc[m][n][2 * j], 0.f) * acc[m][n][8 + 2 * j], fmaxf(acc[m][n][2 * j + 1], 0.f) * acc[m][n][9 + 2 * j]);
        const u32 off = (((tok >> 6) * 54 + (f16 >> 1)) * 4 + ((tok >> 4) & 3)) * 1024 + ((2 * (f16 & 1) + hh) * 16 + (tok & 15)) * 16;
        *(u32x4*)(ws + WS_HF + off) = o;
      }
    }
  } else if (TYPE == 2) {
    const int tok = rb * 256 + row0 + r31;
#pragma unroll
    for (int n = 0; n < NT; n++)
#pragma unroll
      for (int h2 = 0; h2 < 2; h2++) {
        u32x4 o;
#pragma unroll
        for (int j = 0; j < 4; j++) o[j] = pk2(phi(acc[0][n][8 * h2 + 2 * j]), phi(acc[0][n][8 * h2 + 2 * j + 1]));
        const u32 off = ((tok >> 4) * 20 + cb * 5 + n) * 1024 + ((2 * h2 + hh) * 16 + (tok & 15)) * 16;
        *(u32x4*)(ws + WS_QF + off) = o;
      }
  } else {
#pragma unroll
    for (int n = 0; n < NT; n++)
#pragma unroll
      for (int h2 = 0; h2 < 2; h2++) {
        u32x4 o;
#pragma unroll
        for (int j = 0; j < 4; j++) {
          float a = acc[0][n][8 * h2 + 2 * j], b = acc[0][n][8 * h2 + 2 * j + 1];
          if (n < 2) { a = phi(a); b = phi(b); }
          else if (n == 2) { a = b16 ? a : phi(a); b = b16 ? b : phi(b); }
          o[j] = pk2(a, b);
        }
        *(LAS u32x4*)(smem + (wave * 10 + 2 * n + b16) * 1024 + ((2 * h2 + hh) * 16 + (lane & 15)) * 16) = o;
      }
    WAIT_L0(); BAR();
    if (wave < 5) {
      const int i = wave, j0 = i > 0 ? i - 1 : 0, j2 = i < 4 ? i + 1 : 4;
      f32x4 c0 = {0, 0, 0, 0}, c1 = c0, c2 = c0, cz = c0;
      const u32x4 ones = {0x3f803f80u, 0x3f803f80u, 0x3f803f80u, 0x3f803f80u};
#pragma unroll
      for (int s = 0; s < 8; s++) {
        const u32 b = s * 10240 + lane * 16;
        u32x4 a = *(LAS u32x4*)(smem + b + i * 1024);
        u32x4 b0 = *(LAS u32x4*)(smem + b + (5 + j0) * 1024), b1 = *(LAS u32x4*)(smem + b + (5 + i) * 1024), b2 = *(LAS u32x4*)(smem + b + (5 + j2) * 1024);
        c0 = MFMA16(a, b0, c0); c1 = MFMA16(a, b1, c1); c2 = MFMA16(a, b2, c2); cz = MFMA16(a, ones, cz);
      }
      const int d = 16 * i + 4 * (lane >> 4), hd = d / 20, dl = d - hd * 20;
      char* dst = ws + WS_KVP + (cb * 8 + rb) * 6720 + hd * 1680 + dl * 4;
      const int e0 = 16 * (i - 1) + (lane & 15) - hd * 20, e1 = e0 + 16, e2 = e0 + 32;
      if (i > 0 && e0 >= 0 && e0 < 20) *(f32x4*)(dst + e0 * 80) = c0;
      if (e1 >= 0 && e1 < 20) *(f32x4*)(dst + e1 * 80) = c1;
      if (i < 4 && e2 >= 0 && e2 < 20) *(f32x4*)(dst + e2 * 80) = c2;
      if ((lane & 15) == 0) *(f32x4*)(dst + 1600) = cz;
    }
  }
}

DI void k1_pretile(const u16* wd, const u16* wo, int sp, char* ws) {
  const int tid = sp * 512 + threadIdx.x;
  if (tid < 256) ((u32*)(ws + WS_FLAG))[tid * 32] = 0;
  u32x4 v[3][8];
  u32 dsto[3];
#pragma unroll
  for (int it = 0; it < 3; it++) {
    int u = tid + it * 8192;
    u = u < 23680 ? u : 23679;
    const bool isd = u < 17280;
    const int uu = isd ? u : u - 17280, ko = uu / 80, no = uu - ko * 80, ks = ko >> 2, g = ko & 3;
    int rlo, rhi;
    if (isd) { rlo = 32 * ks + 16 * (g >> 1) + 4 * (g & 1); rhi = rlo + 8; }
    else {
      const int pr = ks / 5, s = ks - pr * 5;
      if (s < 4) { rlo = 160 * pr + 80 * (s >> 1) + 32 * (s & 1) + 4 * g; rhi = rlo + 16; }
      else { rlo = 160 * pr + 64 + 4 * g; rhi = rlo + 80; }
    }
    const u16* src = (isd ? wd : wo) + no * 8;
#pragma unroll
    for (int j = 0; j < 8; j++) v[it][j] = *(const u32x4*)(src + ((j < 4 ? rlo : rhi) + (j & 3)) * D_);
    const int cbk = no / 10, nn = no - cbk * 10;
    dsto[it] = (u32)(isd ? WS_WDF + (cbk * 54 + ks) * 5120 : WS_WOF + (cbk * 20 + ks) * 5120) + (nn >> 1) * 1024 + (g * 16 + (nn & 1) * 8) * 16;
  }
#pragma unroll
  for (int it = 0; it < 3; it++) {
#pragma unroll
    for (int c = 0; c < 8; c++) {
      u32x4 o;
#pragma unroll
      for (int j = 0; j < 4; j++)
        o[j] = __builtin_amdgcn_perm(v[it][2 * j + 1][c >> 1], v[it][2 * j][c >> 1], (c & 1) ? 0x07060302u : 0x05040100u);
      *(u32x4*)(ws + dsto[it] + c * 16) = o;
    }
  }
}

__global__ __launch_bounds__(512, 2) void k1_gemm(const u16* __restrict__ wqkv, const u16* __restrict__ wo, const u16* __restrict__ wg,
                                                  const u16* __restrict__ wu, const u16* __restrict__ wd, char* __restrict__ ws) {
  extern __shared__ __attribute__((aligned(16))) char smem[];
  STAMP_INIT(); STAMP(ws, 1, 0);
  const int xcd = blockIdx.x & 7, idx = blockIdx.x >> 3;
  int rb, c, sp;
  if (K1_RB == 1) { rb = xcd; c = idx; sp = (idx - 30) * 8 + xcd; }
  else if (K1_RB == 2) { rb = (xcd >> 1) * 2 + (idx & 1); c = (idx >> 1) * 2 + (xcd & 1); sp = (idx - 30) * 8 + xcd; }
  else if (K1_RB == 4) { rb = (xcd >> 2) * 4 + (idx & 3); c = (idx >> 2) * 4 + (xcd & 3); sp = (c - 30) * 8 + rb; }
  else { rb = idx & 7; c = (idx >> 3) * 8 + xcd; sp = (c - 30) * 8 + rb; }
  const u16* xn = (const u16*)(ws + WS_XN);
  if (c >= 30) k1_pretile(wd, wo, sp, ws);
  else if (c < 18) k1_body<0>(smem, xn, wg + c * 96, wu + c * 96, (D_ * F_ - c * 96) * 2, (D_ * F_ - c * 96) * 2, F_ * 2, rb, c, ws SP_PASS);
  else if (c < 26) {
    const int hg = c - 18, o = 80 * hg - 16 * (hg & 1);
    k1_body<1>(smem, xn, wqkv + 640 + o, wqkv + 1280 + o, (D_ * 1920 - 640 - o) * 2, (D_ * 1920 - 1280 - o) * 2, 3840, rb, hg, ws SP_PASS);
  } else {
    const int qb = c - 26;
    k1_body<2>(smem, xn, wqkv + 160 * qb, wqkv + 160 * qb + 96, (D_ * 1920 - 160 * qb) * 2, (D_ * 1920 - 160 * qb - 96) * 2, 3840, rb, qb, ws SP_PASS);
  }
  STAMP(ws, 1, 3); STAMP_FLUSH(ws, 1);
}

constexpr int K2_KV = 0;
constexpr int K2_CNT = 3376;
constexpr int K2_S = 4096;
constexpr int K2_D = K2_S + 81920;
constexpr int K2_LDS = K2_D + 61440;
#define K2_DEPTH 4

__global__ __launch_bounds__(512, 2) void k2_gemm(const u16* __restrict__ x, u16* __restrict__ out, char* __restrict__ ws) {
  extern __shared__ __attribute__((aligned(16))) char smem[];
  STAMP_INIT(); STAMP(ws, 2, 0);
  const int lane = threadIdx.x & 63;
  const int wave = __builtin_amdgcn_readfirstlane(threadIdx.x >> 6);
  const int xcd = blockIdx.x & 7, idx = blockIdx.x >> 3;
  const int rb = xcd * 4 + (idx >> 3), cb = idx & 7;
  const int g = lane >> 4, c15 = lane & 15;
  const auto wsr = __builtin_amdgcn_make_buffer_rsrc(ws, 0, (int)WS_END, 0x00020000);
  f32x4 kp[8];
  {
    const int q = threadIdx.x < 420 ? threadIdx.x : 419;
#pragma unroll
    for (int p = 0; p < 8; p++)
      kp[p] = __builtin_bit_cast(f32x4, __builtin_amdgcn_raw_buffer_load_b128(wsr, q * 16, (int)WS_KVP + (cb * 8 + p) * 6720, 0));
  }
  auto kv_to_lds = [&]() {
    f32x4 s = kp[0];
#pragma unroll
    for (int p = 1; p < 8; p++) s += kp[p];
    u32x2 o = {pk2(s[0], s[1]), pk2(s[2], s[3])};
    if (threadIdx.x >= 420) o = u32x2{0, 0};
    if (threadIdx.x < 424) *(LAS u32x2*)(smem + K2_KV + threadIdx.x * 8) = o;
    WAIT_L0(); BAR();
  };
  f32x4 acc[4][5];
#pragma unroll
  for (int t = 0; t < 4; t++)
#pragma unroll
    for (int n = 0; n < 5; n++) acc[t][n] = f32x4{0, 0, 0, 0};

  if (wave < 4) {
    const int k0 = wave < 2 ? wave * 14 : 2 + wave * 13;
    const int ha = (int)WS_HF + (rb * 54 + k0) * 4096, wa = (int)WS_WDF + (cb * 54 + k0) * 5120;
    const int last = wave < 2 ? 0 : 0x40000000;
    u32x4 a[K2_DEPTH][4], b[K2_DEPTH][5];
    auto ld = [&](int s) {
      const int o = s == 13 ? last : 0;
#pragma unroll
      for (int t = 0; t < 4; t++) a[s % K2_DEPTH][t] = __builtin_amdgcn_raw_buffer_load_b128(wsr, lane * 16 + o + t * 1024, ha + s * 4096, 0);
#pragma unroll
      for (int n = 0; n < 5; n++) b[s % K2_DEPTH][n] = __builtin_amdgcn_raw_buffer_load_b128(wsr, lane * 16 + o + (n & 3) * 1024, wa + s * 5120 + (n >> 2) * 4096, 0);
    };
#pragma unroll
    for (int s = 0; s < K2_DEPTH - 1; s++) ld(s);
    SCHED();
    kv_to_lds();
    STAMP(ws, 2, 4);
    SCHED();
    ld(K2_DEPTH - 1);
    u32x2 xr[5];
    const int tok = rb * 64 + wave * 16 + c15;
#pragma unroll
    for (int s = 0; s < 14; s++) {
      SCHED();
#pragma unroll
      for (int t = 0; t < 4; t++)
#pragma unroll
        for (int n = 0; n < 5; n++) acc[t][n] = MFMA16(b[s % K2_DEPTH][n], a[s % K2_DEPTH][t], acc[t][n]);
      SCHED();
      if (s == 0) STAMP(ws, 2, 5);
      if (s == 4) STAMP(ws, 2, 6);
      if (s == 9) STAMP(ws, 2, 7);
      if (s + K2_DEPTH < 14) ld(s + K2_DEPTH);
      if (s + K2_DEPTH == 14) {
#pragma unroll
        for (int n = 0; n < 5; n++) xr[n] = *(const u32x2*)(x + tok * D_ + cb * 80 + n * 16 + g * 4);
      }
    }
    STAMP(ws, 2, 1);
#pragma unroll
    for (int t = 0; t < 4; t++)
      if (t != wave) {
        const int slot = t < wave ? t : t - 1;
#pragma unroll
        for (int n = 0; n < 5; n++) *(LAS f32x4*)(smem + K2_D + ((wave * 3 + slot) * 5 + n) * 1024 + lane * 16) = acc[t][n];
      }
    f32x4 own[5];
#pragma unroll
    for (int t = 0; t < 4; t++)
      if (t == wave) {
#pragma unroll
        for (int n = 0; n < 5; n++) own[n] = acc[t][n];
      }
    WAIT_L0(); BAR();
    STAMP(ws, 2, 2);
#pragma unroll
    for (int n = 0; n < 5; n++) {
      f32x4 bsum = own[n], ssum = {0, 0, 0, 0};
#pragma unroll
      for (int w = 0; w < 3; w++) {
        const int src = w < wave ? w : w + 1, slot = wave < src ? wave : wave - 1;
        bsum += *(LAS f32x4*)(smem + K2_D + ((src * 3 + slot) * 5 + n) * 1024 + lane * 16);
      }
#pragma unroll
      for (int w = 0; w < 4; w++) ssum += *(LAS f32x4*)(smem + K2_S + ((w * 4 + wave) * 5 + n) * 1024 + lane * 16);
      float r[4];
#pragma unroll
      for (int j = 0; j < 4; j++) {
        const float v = bsum[j], gt = v * __builtin_amdgcn_rcpf(1.f + __builtin_amdgcn_exp2f(-1.44269504089f * v));
        const float xv = (j & 1) ? bhi(xr[n][j >> 1]) : blo(xr[n][j >> 1]);
        r[j] = xv + ssum[j] * gt;
      }
      *(u32x2*)(out + tok * D_ + cb * 80 + n * 16 + g * 4) = u32x2{pk2(r[0], r[1]), pk2(r[2], r[3])};
    }
  } else {
    const int w = wave - 4, odd = cb & 1;
    u32x4 pq[3], wf[5][5];
    const int qa = (int)WS_QF + ((rb * 4 + w) * 20 + (cb >> 1) * 5 + 2 * odd) * 1024;
#pragma unroll
    for (int s = 0; s < 3; s++) pq[s] = __builtin_amdgcn_raw_buffer_load_b128(wsr, lane * 16 + s * 1024, qa, 0);
    const int wa = (int)WS_WOF + (cb * 20 + w * 5) * 5120;
#pragma unroll
    for (int s = 0; s < 5; s++)
#pragma unroll
      for (int n = 0; n < 5; n++) wf[s][n] = __builtin_amdgcn_raw_buffer_load_b128(wsr, lane * 16 + (n & 3) * 1024, wa + s * 5120 + (n >> 2) * 4096, 0);
    SCHED();
    kv_to_lds();
    SCHED();
    f32x4 on[5], dn = {0, 0, 0, 0};
#pragma unroll
    for (int j = 0; j < 5; j++) on[j] = dn;
    auto kvfrag = [&](int hd, int row, int s) {
      const int d0 = 32 * s - 16 * odd + 16 * (g >> 1) + 4 * (g & 1) - 20 * hd, d1 = d0 + 8;
      const int base = hd * 840 + row * 40;
      const u32x2 lo = *(LAS u32x2*)(smem + K2_KV + ((d0 >= 0 && d0 < 20) ? base + d0 * 2 : 3360));
      const u32x2 hi = *(LAS u32x2*)(smem + K2_KV + ((d1 >= 0 && d1 < 20) ? base + d1 * 2 : 3360));
      return u32x4{lo[0], lo[1], hi[0], hi[1]};
    };
#pragma unroll
    for (int s = 0; s < 3; s++) {
#pragma unroll
      for (int j = 0; j < 5; j++) {
        constexpr int lo_[5] = {0, 0, 20, 40, 60}, hi_[5] = {20, 40, 60, 80, 80};
        if (32 * s - 16 >= hi_[j] || 32 * s + 32 <= lo_[j]) continue;
        const int e = 16 * j + c15, hd = e / 20;
        on[j] = MFMA16(kvfrag(hd, e - hd * 20, s), pq[s], on[j]);
      }
      dn = MFMA16(kvfrag(c15 & 3, 20, s), pq[s], dn);
    }
    float rd[4];
#pragma unroll
    for (int j = 0; j < 4; j++) rd[j] = __builtin_amdgcn_rcpf(dn[j] + 1e-6f);
    const float rs[5] = {rd[0], g == 0 ? rd[0] : rd[1], g < 2 ? rd[1] : rd[2], g < 3 ? rd[2] : rd[3], rd[3]};
    u32 op[5][2];
#pragma unroll
    for (int j = 0; j < 5; j++) { op[j][0] = pk2(on[j][0] * rs[j], on[j][1] * rs[j]); op[j][1] = pk2(on[j][2] * rs[j], on[j][3] * rs[j]); }
    const int oa = (int)WS_OF + ((rb * 8 + cb) * 4 + w) * 2560;
    __builtin_amdgcn_raw_buffer_store_b128(u32x4{op[0][0], op[0][1], op[1][0], op[1][1]}, wsr, lane * 16, oa, 16);
    __builtin_amdgcn_raw_buffer_store_b128(u32x4{op[2][0], op[2][1], op[3][0], op[3][1]}, wsr, lane * 16 + 1024, oa, 16);
    __builtin_amdgcn_raw_buffer_store_b64(u32x2{op[4][0], op[4][1]}, wsr, lane * 8 + 2048, oa, 16);
    WAIT_V(0);
    GAS u32* flags = (GAS u32*)(ws + WS_FLAG);
    if (lane == 0) {
      const u32 n = __hip_atomic_fetch_add((LAS u32*)(smem + K2_CNT), 1u, __ATOMIC_RELAXED, __HIP_MEMORY_SCOPE_WORKGROUP);
      if (n == 3) __hip_atomic_store(flags + (rb * 8 + cb) * 32, 1u, __ATOMIC_RELAXED, __HIP_MEMORY_SCOPE_AGENT);
    }
    STAMP(ws, 2, 1);
    {
      GAS u32* f = flags + (rb * 8 + 2 * w + (lane & 1)) * 32;
      for (u32 spins = 0; spins < (1u << 22); spins++) {
        const u32 v = __hip_atomic_load(f, __ATOMIC_RELAXED, __HIP_MEMORY_SCOPE_AGENT);
        if (__all(v == 1u)) break;
        __builtin_amdgcn_s_sleep(1);
      }
      __builtin_amdgcn_fence(__ATOMIC_ACQUIRE, "wavefront");
    }
    STAMP(ws, 2, 2);
    const int ob = (int)WS_OF + (rb * 8 + 2 * w) * 4 * 2560;
#pragma unroll
    for (int th = 0; th < 2; th++) {
      u32x4 of[2][5];
#pragma unroll
      for (int t = 0; t < 2; t++) {
        const int o = ob + (2 * th + t) * 2560;
#pragma unroll
        for (int s = 0; s < 4; s++) of[t][s] = __builtin_amdgcn_raw_buffer_load_b128(wsr, lane * 16 + (s & 1) * 1024, o + (s >> 1) * 10240, 16);
        const u32x2 lo = __builtin_amdgcn_raw_buffer_load_b64(wsr, lane * 8 + 2048, o, 16);
        const u32x2 hi = __builtin_amdgcn_raw_buffer_load_b64(wsr, lane * 8 + 2048, o + 10240, 16);
        of[t][4] = u32x4{lo[0], lo[1], hi[0], hi[1]};
      }
#pragma unroll
      for (int t = 0; t < 2; t++)
#pragma unroll
        for (int s = 0; s < 5; s++)
#pragma unroll
          for (int n = 0; n < 5; n++) acc[2 * th + t][n] = MFMA16(wf[s][n], of[t][s], acc[2 * th + t][n]);
    }
#pragma unroll
    for (int t = 0; t < 4; t++)
#pragma unroll
      for (int n = 0; n < 5; n++) *(LAS f32x4*)(smem + K2_S + ((w * 4 + t) * 5 + n) * 1024 + lane * 16) = acc[t][n];
    STAMP(ws, 2, 4);
    WAIT_L0(); BAR();
  }
  STAMP(ws, 2, 3); STAMP_FLUSH(ws, 2);
}

extern "C" void kernel_launch(void* const* d_in, const int* in_sizes, int n_in, void* d_out, int out_size, void* d_ws, size_t ws_size,
                              hipStream_t stream) {
  const u16* x = (const u16*)d_in[0];
  const u16* wqkv = (const u16*)d_in[1];
  const u16* wo = (const u16*)d_in[2];
  const u16* wg = (const u16*)d_in[4];
  const u16* wu = (const u16*)d_in[5];
  const u16* wd = (const u16*)d_in[6];
  char* ws = (char*)d_ws;
  (void)hipFuncSetAttribute((const void*)k1_gemm, hipFuncAttributeMaxDynamicSharedMemorySize, K1_LDS);
  (void)hipFuncSetAttribute((const void*)k2_gemm, hipFuncAttributeMaxDynamicSharedMemorySize, K2_LDS);
  k0_ln<<<256, 128, 0, stream>>>(x, ws);
  k1_gemm<<<256, 512, K1_LDS, stream>>>(wqkv, wo, wg, wu, wd, ws);
  k2_gemm<<<256, 512, K2_LDS, stream>>>(x, (u16*)d_out, ws);
}
```

```cpp
#include <hip/hip_runtime.h>
#include <type_traits>
typedef unsigned short u16;
typedef unsigned int u32;
typedef unsigned long long u64;
typedef short s16x4 __attribute__((ext_vector_type(4)));
typedef short s16x8 __attribute__((ext_vector_type(8)));
typedef __bf16 bf16x8 __attribute__((ext_vector_type(8)));
typedef __bf16 bf16x2 __attribute__((ext_vector_type(2)));
typedef float f32x2 __attribute__((ext_vector_type(2)));
typedef float f32x4 __attribute__((ext_vector_type(4)));
typedef u32 u32x2 __attribute__((ext_vector_type(2)));
typedef u32 u32x4 __attribute__((ext_vector_type(4)));
#define LAS __attribute__((address_space(3)))
#define GAS __attribute__((address_space(1)))
#define DI __device__ __forceinline__

constexpr int S_ = 2048, D_ = 640, F_ = 1728;
constexpr int WS_EPOCH = 0;
constexpr int WS_F1   = 4096;
constexpr int WS_FO   = WS_F1 + 256 * 128;
constexpr int WS_FP   = WS_FO + 256 * 128;
constexpr int WS_STAT = WS_FP + 256 * 128;
constexpr int WS_KVP  = WS_STAT + 2048 * 8;
constexpr int WS_HF   = 1 << 20;
constexpr int WS_QF   = WS_HF + 32 * 54 * 4096;
constexpr int WS_OF   = WS_QF + 128 * 20 * 1024;
constexpr int WS_WDF  = WS_OF + 128 * 20 * 1024;
constexpr int WS_WOF  = WS_WDF + 8 * 54 * 5120;
constexpr int WS_END  = WS_WOF + 8 * 20 * 5120;
constexpr size_t WS_DBG = 64u << 20;
constexpr u64 TAG_STEP = 0x9E3779B97F4A7C15ull;
constexpr u32 SPIN_CAP = 1u << 16;
constexpr int SC1 = 16;

constexpr int L_X0 = 0, L_W1 = 32768, L_W2 = 57344, L_X1 = 81920, L_W0 = 114688, L_SP = 139264;
constexpr int W_IMG1 = 12288;
constexpr int L_P = 0;
constexpr int L_Q = 81920;
constexpr int L_KV = 143360;
constexpr int L_KVOP = 79872;
constexpr int L_CS = 161792;
constexpr int L_CNT = 163328;
constexpr int LDS_BYTES = 163840;

DI u32 pk2(float a, float b) { f32x2 v = {a, b}; return __builtin_bit_cast(u32, __builtin_convertvector(v, bf16x2)); }
DI float blo(u32 d) { return __uint_as_float(d << 16); }
DI float bhi(u32 d) { return __uint_as_float(d & 0xffff0000u); }
DI float rbf(float f) { return blo(pk2(f, f)); }
DI u32x4 mk_srd(const void* p, u32 bytes) {
  u64 a = (u64)p;
  u32x4 r;
  r[0] = __builtin_amdgcn_readfirstlane((u32)a); r[1] = __builtin_amdgcn_readfirstlane((u32)(a >> 32) & 0xffffu);
  r[2] = __builtin_amdgcn_readfirstlane(bytes); r[3] = 0x00020000u;
  return r;
}
#define DMA16(m0v, voff, srd, soff) \
  asm volatile("s_mov_b32 m0, %0\n\ts_nop 0\n\tbuffer_load_dwordx4 %1, %2, %3 offen lds" ::"s"(m0v), "v"(voff), "s"(srd), "s"(soff) : "memory")
#define DMA16C(m0v, voff, srd, soff) \
  asm volatile("s_mov_b32 m0, %0\n\ts_nop 0\n\tbuffer_load_dwordx4 %1, %2, %3 offen sc1 lds" ::"s"(m0v), "v"(voff), "s"(srd), "s"(soff) : "memory")
#define WAIT_V(n) asm volatile("s_waitcnt vmcnt(" #n ")" ::: "memory")
#define WAIT_L0() do { asm volatile("" ::: "memory"); __builtin_amdgcn_s_waitcnt(0xC07F); asm volatile("" ::: "memory"); } while (0)
#define BAR() __builtin_amdgcn_s_barrier()
#define SCHED() __builtin_amdgcn_sched_barrier(0)
#define MFMA16(a, b, c) __builtin_amdgcn_mfma_f32_16x16x32_bf16(__builtin_bit_cast(bf16x8, a), __builtin_bit_cast(bf16x8, b), c, 0, 0, 0)
#define LD128(r, v, s, aux) __builtin_amdgcn_raw_buffer_load_b128(r, v, s, aux)
#define LD64(r, v, s, aux) __builtin_amdgcn_raw_buffer_load_b64(r, v, s, aux)
#define ST128(x, r, v, s) __builtin_amdgcn_raw_buffer_store_b128(x, r, (v) + (s), 0, SC1)
#define ST64(x, r, v, s) __builtin_amdgcn_raw_buffer_store_b64(x, r, (v) + (s), 0, SC1)
typedef __amdgpu_buffer_rsrc_t rsrc_t;

#define STAMP_INIT() do {} while (0)
#define STAMP(i) do {} while (0)
#define STAMP_FLUSH(ws) do {} while (0)
#define SP_ARG
#define SP_PASS

DI u64 flag_ld(char* ws, u32 off) { return __hip_atomic_load((GAS u64*)(ws + off), __ATOMIC_RELAXED, __HIP_MEMORY_SCOPE_AGENT); }
DI void flag_st(char* ws, u32 off, u64 v) { __hip_atomic_store((GAS u64*)(ws + off), v, __ATOMIC_RELAXED, __HIP_MEMORY_SCOPE_AGENT); }
DI void flag_wait(char* ws, u32 off, u64 tag, u64 v) {
  for (u32 spins = 0; !__all(v == tag) && spins < SPIN_CAP; spins++) {
    __builtin_amdgcn_s_sleep(1);
    v = flag_ld(ws, off);
  }
  __builtin_amdgcn_fence(__ATOMIC_ACQUIRE, "wavefront");
}
DI void lds_wait(char* smem, int off, u32 want) {
  for (u32 spins = 0; spins < SPIN_CAP; spins++) {
    const u32 v = __hip_atomic_load((LAS u32*)(smem + off), __ATOMIC_RELAXED, __HIP_MEMORY_SCOPE_WORKGROUP);
    if (__builtin_amdgcn_readfirstlane(v) >= want) break;
    __builtin_amdgcn_s_sleep(1);
  }
  asm volatile("" ::: "memory");
}
DI void arrive(char* smem, int cnt, u32 n, char* ws, u32 flag, u64 tag) {
  WAIT_V(0);
  if ((threadIdx.x & 63) == 0) {
    const u32 k = __hip_atomic_fetch_add((LAS u32*)(smem + cnt), 1u, __ATOMIC_RELAXED, __HIP_MEMORY_SCOPE_WORKGROUP);
    if (k == n - 1) flag_st(ws, flag, tag);
  }
}
DI u64 get_tag(char* ws) {
  u64 e;
  asm volatile("s_load_dwordx2 %0, %1, 0x0\n\ts_waitcnt lgkmcnt(0)" : "=s"(e) : "s"(ws + WS_EPOCH) : "memory");
  return e + TAG_STEP;
}
DI float phi(float q) {
  const float e = fminf(__builtin_amdgcn_exp2f(q * 1.44269504089f), 1.f);
  return fmaxf(q, 0.f) + 1e-6f + e;
}
DI int d_k0(int w) { return w < 2 ? w * 14 : 2 + w * 13; }
DI u32 p_src(int cb2, int i) { const int w = i / 20; return WS_WDF + (cb2 * 54 + d_k0(w) + 4) * 5120 + (i - w * 20) * 1024; }

template <int TYPE>
DI void phase1(char* smem, const u16* xa, const u16* wd, const u16* wo, const u16* w0, const u16* w1, u32 wbytes0, u32 wbytes1, int pitch, int o0, int o1, int rb, int cb,
               int c, int cb2, char* ws, u64& tag SP_ARG) {
  constexpr int NT = TYPE == 0 ? 6 : 5;
  const int lane = threadIdx.x & 63;
  const int wave = __builtin_amdgcn_readfirstlane(threadIdx.x >> 6);
  const int grp = wave >> 2, wg = wave & 3, wm = wave >> 1, wn = wave & 1;
  const u32 lds0 = (u32)(size_t)(LAS char*)smem;
  const u32x4 srd = grp == 0 ? mk_srd(xa + (size_t)rb * 256 * D_, 256 * D_ * 2) : (wg >> 1) ? mk_srd(w1, wbytes1) : mk_srd(w0, wbytes0);
  u32 vo[6];
  if (grp == 0) {
#pragma unroll
    for (int i = 0; i < 2; i++) { int r = 8 * i + (lane >> 3); vo[i] = (64 * wg + r) * (D_ * 2) + (((lane & 7) ^ ((r >> 1) & 7)) << 4); }
    vo[2] = vo[3] = vo[4] = vo[5] = 0;
  } else {
#pragma unroll
    for (int i = 0; i < 6; i++) { int o = (6 * (wg & 1) + i) * 1024 + lane * 16, r = o / 192; vo[i] = r * pitch + ((o - r * 192) ^ ((r & 8) << 2)); }
  }
  const u32 mb = lds0 + wg * (grp == 0 ? 8192 : 6144);
  auto dma_x = [&](int i, u32 m, u32 so) { DMA16(m + i * 1024, vo[i & 1], srd, so + (i >> 1) * 16 * D_ * 2); };
  auto dma_w = [&](int i, u32 m, u32 so) { DMA16(m + i * 1024, vo[i], srd, so); };
  const int unit = (c * 8 + rb) * 4 + wg, uks = (unit < 740 ? unit : 739) / 10, ucg = (unit < 740 ? unit : 739) - uks * 10;
  asm volatile("s_nop 4" ::: "memory");
  if (grp == 0) {
    int r0, r1;
    if (uks < 54) { r0 = 32 * uks; r1 = r0 + 16; }
    else {
      const int k = uks - 54, pr = k / 5, s5 = k - pr * 5;
      if (s5 < 4) { r0 = 160 * pr + 80 * (s5 >> 1) + 32 * (s5 & 1); r1 = r0 + 16; } else { r0 = 160 * pr + 64; r1 = r0 + 80; }
    }
    const u32x4 srdu = uks < 54 ? mk_srd(wd, F_ * D_ * 2) : mk_srd(wo, unit < 740 ? D_ * D_ * 2 : 0);
    const u32 vu = (lane >> 3) * (D_ * 2) + (lane & 7) * 16;
    asm volatile("s_nop 4" ::: "memory");
#pragma unroll
    for (int i = 0; i < 4; i++) DMA16(lds0 + L_SP + wg * 4096 + i * 1024, vu, srdu, ((i < 2 ? r0 : r1) + 8 * (i & 1)) * (D_ * 2) + ucg * 128);
#pragma unroll
    for (int t = 0; t < 2; t++)
#pragma unroll
      for (int i = 0; i < 8; i++) dma_x(i, mb + (t ? L_X1 : L_X0), t * 128);
  } else {
#pragma unroll
    for (int t = 0; t < 3; t++)
#pragma unroll
      for (int i = 0; i < 6; i++) dma_w(i, mb + (t == 0 ? L_W0 : t == 1 ? L_W1 : L_W2), t * 64 * pitch);
  }
  if (threadIdx.x < 16) ((LAS u32*)(smem + L_CNT))[threadIdx.x] = 0;
  const int g = lane >> 4, c15 = lane & 15;
  u32 ax[2], aw[2][2];
#pragma unroll
  for (int k2 = 0; k2 < 2; k2++) {
    const int ke = k2 ^ (wm & 1);
    ax[k2] = lds0 + (wm * 64 + c15) * 128 + (((4 * ke + g) ^ (c15 >> 1)) << 4);
    const int c0 = TYPE == 0 ? 3 * wn : (wn ? o1 : o0);
    const u32 awc = lds0 + (TYPE == 0 ? 0 : wn * W_IMG1) + (32 * ke + 8 * g + (c15 >> 2)) * 192 + (lane & 3) * 8;
    aw[k2][0] = awc + (c0 ^ (g & 1)) * 32;
    aw[k2][1] = awc + ((c0 + 1) ^ (g & 1)) * 32;
  }
  f32x4 acc[4][NT];
#pragma unroll
  for (int m = 0; m < 4; m++)
#pragma unroll
    for (int n = 0; n < NT; n++) acc[m][n] = f32x4{0, 0, 0, 0};
  s16x8 xf[2][4], wf[2][NT];
  float cs[3] = {0.f, 0.f, 0.f};
  const u32x4 srdp = mk_srd(ws, WS_END);
  const u32 l16 = lane * 16;

  auto trd = [&](LAS char* p) { return __builtin_amdgcn_ds_read_tr16_b64_v4i16((LAS s16x4*)p); };
  auto dma_p = [&](int i) { DMA16C(lds0 + L_P + i * 1024, l16, srdp, p_src(cb2, i)); };
  auto load_phase = [&](auto mode, u32 xo, u32 wo, u32 m, u32 so) {
    constexpr int MODE = decltype(mode)::value;
    auto dma = [&](int i) {
      if (MODE == 1) dma_x(i, m, so);
      if (MODE == 2) dma_w(i, m, so);
      if (MODE >= 3 && (TYPE != 1 || i < 4 || so + i < 78)) dma_p(so + i);
    };
#pragma unroll
    for (int k2 = 0; k2 < 2; k2++) {
      u32 b0 = aw[k2][0] + wo, b1 = aw[k2][1] + wo, bx = ax[k2] + xo;
      asm volatile("" : "+v"(b0), "+v"(b1), "+v"(bx));
      if (MODE == 1 || MODE == 3) dma(4 * k2);
#pragma unroll
      for (int mm = 0; mm < 4; mm++) xf[k2][mm] = *(LAS s16x8*)((LAS char*)(size_t)bx + mm * 2048);
#pragma unroll
      for (int n = 0; n < NT; n++) {
        if (!(n & 1)) dma((MODE & 1) ? 4 * k2 + 1 + n / 2 : 3 * k2 + n / 2);
        const int nn = TYPE == 0 ? n % 3 : n;
        LAS char* a = (LAS char*)(size_t)((nn & 1) ? b1 : b0) + (nn & ~1) * 32 + (TYPE == 0 && n >= 3 ? W_IMG1 : 0);
        s16x4 lo = trd(a), hi = trd(a + 4 * 192);
        wf[k2][n] = __builtin_shufflevector(lo, hi, 0, 1, 2, 3, 4, 5, 6, 7);
      }
    }
  };
  auto colsum = [&](auto half) {
    constexpr int H = decltype(half)::value;
#pragma unroll
    for (int n = 3 * H; n < (H ? NT : 3); n++) {
      const u32x4 d = __builtin_bit_cast(u32x4, wf[0][n]);
      float s0 = blo(d[0]) + bhi(d[0]), s1 = blo(d[1]) + bhi(d[1]), s2 = blo(d[2]) + bhi(d[2]), s3 = blo(d[3]) + bhi(d[3]);
      cs[n - 3 * H] += (s0 + s1) + (s2 + s3);
    }
  };
  auto compute = [&]() {
#pragma unroll
    for (int k2 = 0; k2 < 2; k2++)
#pragma unroll
      for (int m = 0; m < 4; m++)
#pragma unroll
        for (int n = 0; n < NT; n++)
          acc[m][n] = TYPE == 1 ? MFMA16(xf[k2][m], wf[k2][n], acc[m][n]) : MFMA16(wf[k2][n], xf[k2][m], acc[m][n]);
  };
  auto interleave = [&]() {
#pragma unroll
    for (int i = 0; i < 8 * NT; i++) { __builtin_amdgcn_sched_group_barrier(0x8, 1, 0); __builtin_amdgcn_sched_group_barrier(0x2, 1, 0); }
  };
  auto cs_out = [&](auto half) {
    constexpr int H = decltype(half)::value;
#pragma unroll
    for (int n = 3 * H; n < (H ? NT : 3); n++) {
      float s = cs[n - 3 * H];
      s += __shfl_xor(s, 16); s += __shfl_xor(s, 32);
      if (lane < 16) ((LAS float*)(smem + L_CS))[(((wm & 1) * 2 + wn) * 6 + n) * 16 + lane] = s;
    }
  };
  using I0 = std::integral_constant<int, 0>; using I1 = std::integral_constant<int, 1>; using I2 = std::integral_constant<int, 2>;
  using I3 = std::integral_constant<int, 3>; using I4 = std::integral_constant<int, 4>;
  asm volatile("" : "+v"(ax[0]), "+v"(ax[1]), "+v"(aw[0][0]), "+v"(aw[0][1]), "+v"(aw[1][0]), "+v"(aw[1][1]));
  const u32 spf = WS_F1 + ((lane & 7) * 32 + 30 + ((lane >> 3) & 1)) * 128;
  const rsrc_t wsr = __builtin_amdgcn_make_buffer_rsrc(ws, 0, WS_END, 0x00020000);
  u32x4 st[TYPE == 1 ? 8 : 2];
  auto ld_stats = [&]() {
    if (TYPE == 1) {
#pragma unroll
      for (int i = 0; i < 8; i++) st[i] = LD128(wsr, g * 32 + (i >> 1) * 128 + (i & 1) * 16, WS_STAT + (rb * 256 + wm * 64) * 8, SC1);
    } else {
#pragma unroll
      for (int i = 0; i < 2; i++) {
        const u32x2 a = LD64(wsr, c15 * 8 + i * 256, WS_STAT + (rb * 256 + wm * 64) * 8, SC1), b = LD64(wsr, c15 * 8 + i * 256 + 128, WS_STAT + (rb * 256 + wm * 64) * 8, SC1);
        st[i] = u32x4{a[0], a[1], b[0], b[1]};
      }
    }
  };
  tag = get_tag(ws);
  if (grp == 0) {
    if (TYPE != 1) STAMP(7);
    WAIT_V(16);
    STAMP(12);
    {
      const bool alt = uks < 54 && uks % 3 == 1;
      const int ra = alt ? 16 * (g >> 1) + 4 * (g & 1) : 4 * g, rd = alt ? 8 : 16;
      LAS char* pa = (LAS char*)smem + L_SP + wg * 4096 + (ra + (c15 >> 2)) * 128 + (lane & 3) * 8;
#pragma unroll
      for (int j = 0; j < 4; j++) {
        const s16x4 lo = trd(pa + j * 32), hi = trd(pa + rd * 128 + j * 32);
        const int n16 = ucg * 4 + j, cbk = n16 / 5, nt = n16 - cbk * 5;
        const int dst = unit >= 740 ? 0x40000000 : (uks < 54 ? WS_WDF + (cbk * 54 + uks) * 5120 : WS_WOF + (cbk * 20 + uks - 54) * 5120) + nt * 1024;
        ST128(__builtin_bit_cast(u32x4, __builtin_shufflevector(lo, hi, 0, 1, 2, 3, 4, 5, 6, 7)), wsr, l16, dst);
      }
    }
    WAIT_L0(); WAIT_V(12); BAR();
    STAMP(1);
    SCHED();
    load_phase(I0{}, L_X0, L_W0, 0, 0); SCHED(); compute(); colsum(I0{}); interleave(); SCHED();
    if (TYPE != 1) STAMP(5);
    arrive(smem, L_CNT + 12, 4, ws, WS_FP + (rb * 32 + c) * 128, tag);
    BAR(); SCHED();
    if (TYPE != 1) STAMP(6);
    u32 w_cur = L_W1, w_nxt = L_W2, w_prv = L_W0;
#pragma unroll 1
    for (int t = 1; t < 8; t++) {
      const u32 xo = (t & 1) * L_X1;
      load_phase(I1{}, xo, w_cur, mb + (L_X1 - xo), (t + 1) * 128);
      { const u32 u = w_prv; w_prv = w_cur; w_cur = w_nxt; w_nxt = u; }
      SCHED(); compute(); colsum(I0{}); interleave(); SCHED(); WAIT_V(0); BAR(); SCHED();
    }
    STAMP(13);
    load_phase(I1{}, L_X0, L_W2, mb + L_X1, 9 * 128);
    u64 fv = flag_ld(ws, spf);
    SCHED(); compute(); colsum(I0{}); interleave(); SCHED(); WAIT_V(0);
    flag_wait(ws, spf, tag, fv);
    BAR(); SCHED();
    STAMP(14);
    load_phase(I3{}, L_X1, L_W0, 0, wg * 8);
    ld_stats();
    SCHED(); compute(); colsum(I0{}); interleave(); SCHED();
    cs_out(I0{}); WAIT_L0(); BAR(); SCHED();
  } else {
    __builtin_amdgcn_s_setprio(1);
#pragma unroll
    for (int m = 0; m < 4; m++)
#pragma unroll
      for (int n = 0; n < NT; n++) asm volatile("" : "+v"(acc[m][n]));
    WAIT_V(12); BAR();
    STAMP(1);
    SCHED();
    load_phase(I0{}, L_X0, L_W0, 0, 0); WAIT_L0(); WAIT_V(6); SCHED(); BAR(); SCHED();
    u32 w_cur = L_W1, w_nxt = L_W2, w_prv = L_W0;
#pragma unroll 1
    for (int t = 1; t < 8; t++) {
      compute(); colsum(I1{}); interleave(); SCHED();
      load_phase(I2{}, (t & 1) * L_X1, w_cur, mb + w_prv, (t + 2) * 64 * pitch);
      { const u32 u = w_prv; w_prv = w_cur; w_cur = w_nxt; w_nxt = u; }
      WAIT_L0(); WAIT_V(6); SCHED(); BAR(); SCHED();
    }
    u64 fv = flag_ld(ws, spf);
    SCHED(); compute(); colsum(I1{}); interleave(); SCHED();
    WAIT_V(0);
    flag_wait(ws, spf, tag, fv);
    load_phase(I4{}, L_X0, L_W2, 0, 32 + wg * 6); WAIT_L0(); SCHED(); BAR(); SCHED();
    compute(); colsum(I1{}); interleave(); SCHED();
    load_phase(I4{}, L_X1, L_W0, 0, 56 + wg * 6);
    ld_stats();
    colsum(I1{}); cs_out(I1{}); WAIT_L0(); SCHED(); BAR(); SCHED();
    compute(); SCHED();
    __builtin_amdgcn_s_setprio(0);
  }
  STAMP(2);

  LAS float* csl = (LAS float*)(smem + L_CS) + wn * 96;
  if (TYPE != 1) {
    float mu[4], rs[4];
#pragma unroll
    for (int m = 0; m < 4; m++) { mu[m] = -__uint_as_float(st[m >> 1][(m & 1) * 2]); rs[m] = __uint_as_float(st[m >> 1][(m & 1) * 2 + 1]); }
#pragma unroll
    for (int n = 0; n < NT; n++) {
      const f32x4 cv = *(LAS f32x4*)(csl + n * 16 + g * 4) + *(LAS f32x4*)(csl + 192 + n * 16 + g * 4);
#pragma unroll
      for (int m = 0; m < 4; m++)
#pragma unroll
        for (int j = 0; j < 4; j++) acc[m][n][j] = __builtin_fmaf(mu[m], cv[j], acc[m][n][j]);
    }
    if (TYPE == 0) {
      u32 hp[4][3][2];
#pragma unroll
      for (int m = 0; m < 4; m++) {
        const float r2 = rs[m] * rs[m];
#pragma unroll
        for (int n = 0; n < 3; n++)
#pragma unroll
          for (int j = 0; j < 2; j++)
            hp[m][n][j] = pk2(fmaxf(acc[m][n][2 * j], 0.f) * r2 * acc[m][(n + 3) % NT][2 * j], fmaxf(acc[m][n][2 * j + 1], 0.f) * r2 * acc[m][(n + 3) % NT][2 * j + 1]);
      }
      const int base = WS_HF + ((rb * 4 + wm) * 54 + cb * 3) * 4096;
#pragma unroll
      for (int m = 0; m < 4; m++) {
        const u32x4 o = wn ? u32x4{hp[m][1][0], hp[m][1][1], hp[m][2][0], hp[m][2][1]} : u32x4{hp[m][0][0], hp[m][0][1], hp[m][1][0], hp[m][1][1]};
        ST128(o, wsr, l16 + m * 1024, base + wn * 8192);
      }
#pragma unroll
      for (int mp = 0; mp < 2; mp++) {
        const u32 a0 = wn ? hp[2 * mp][0][0] : hp[2 * mp][2][0], a1 = wn ? hp[2 * mp][0][1] : hp[2 * mp][2][1];
        const u32 b0 = wn ? hp[2 * mp + 1][0][0] : hp[2 * mp + 1][2][0], b1 = wn ? hp[2 * mp + 1][0][1] : hp[2 * mp + 1][2][1];
        const auto s0 = __builtin_amdgcn_permlane32_swap(a0, b0, false, false), s1 = __builtin_amdgcn_permlane32_swap(a1, b1, false, false);
        ST128((u32x4{s0[0], s1[0], s0[1], s1[1]}), wsr, (lane >> 5) * 1024 + (lane & 31) * 16 + mp * 2048, base + 4096 + wn * 512);
      }
    } else {
#pragma unroll
      for (int m = 0; m < 4; m++) {
        u32 p[5][2];
#pragma unroll
        for (int n = 0; n < 5; n++) {
          p[n][0] = pk2(phi(acc[m][n][0] * rs[m]), phi(acc[m][n][1] * rs[m]));
          p[n][1] = pk2(phi(acc[m][n][2] * rs[m]), phi(acc[m][n][3] * rs[m]));
        }
        const int base = WS_QF + ((rb * 16 + wm * 4 + m) * 8 + cb * 2 + wn) * 2560;
        ST128((u32x4{p[0][0], p[0][1], p[1][0], p[1][1]}), wsr, l16, base);
        ST128((u32x4{p[2][0], p[2][1], p[3][0], p[3][1]}), wsr, l16 + 1024, base);
        ST64((u32x2{p[4][0], p[4][1]}), wsr, lane * 8 + 2048, base);
      }
    }
  } else {
    float cv[5];
#pragma unroll
    for (int n = 0; n < 5; n++) cv[n] = csl[n * 16 + c15] + csl[192 + n * 16 + c15];
#pragma unroll
    for (int m = 0; m < 4; m++)
#pragma unroll
      for (int j = 0; j < 4; j++) {
        const float mu = -__uint_as_float(st[2 * m + (j >> 1)][(j & 1) * 2]), rs = __uint_as_float(st[2 * m + (j >> 1)][(j & 1) * 2 + 1]);
#pragma unroll
        for (int n = 0; n < 5; n++) acc[m][n][j] = __builtin_fmaf(mu, cv[n], acc[m][n][j]) * rs;
      }
    if (wn == 0) {
#pragma unroll
      for (int m = 0; m < 4; m++)
#pragma unroll
        for (int n = 0; n < 5; n++)
#pragma unroll
          for (int j = 0; j < 4; j++) acc[m][n][j] = phi(acc[m][n][j]);
    }
    STAMP(5);
    auto kvo = [&](int s, int tile) { return L_KVOP + (s * 10 + tile) * 1024; };
#pragma unroll
    for (int mp = 0; mp < 2; mp++)
#pragma unroll
      for (int n = 0; n < 5; n++)
        *(LAS u32x4*)(smem + kvo(wm * 2 + mp, wn * 5 + n) + l16) =
            u32x4{pk2(acc[2 * mp][n][0], acc[2 * mp][n][1]), pk2(acc[2 * mp][n][2], acc[2 * mp][n][3]),
                  pk2(acc[2 * mp + 1][n][0], acc[2 * mp + 1][n][1]), pk2(acc[2 * mp + 1][n][2], acc[2 * mp + 1][n][3])};
    WAIT_L0(); STAMP(6); BAR();
    STAMP(7);
    f32x4 c0 = {0, 0, 0, 0}, c1 = c0, c2 = c0, cz = c0;
    const int i = wave, j0 = i > 0 ? i - 1 : 0, j2 = i < 4 ? i + 1 : 4;
    if (wave < 5) {
      const u32x4 ones = {0x3f803f80u, 0x3f803f80u, 0x3f803f80u, 0x3f803f80u};
#pragma unroll
      for (int s = 0; s < 8; s++) {
        u32x4 a = *(LAS u32x4*)(smem + kvo(s, i) + l16);
        u32x4 b0 = *(LAS u32x4*)(smem + kvo(s, 5 + j0) + l16), b1 = *(LAS u32x4*)(smem + kvo(s, 5 + i) + l16), b2 = *(LAS u32x4*)(smem + kvo(s, 5 + j2) + l16);
        c0 = MFMA16(a, b0, c0); c1 = MFMA16(a, b1, c1); c2 = MFMA16(a, b2, c2); cz = MFMA16(a, ones, cz);
      }
      const int d = 16 * i + 4 * g, hd = d / 20, dl = d - hd * 20;
      const int dst = hd * 1680 + dl * 4, so = WS_KVP + (cb * 8 + rb) * 6720, oob = 0x40000000;
      const int e0 = 16 * (i - 1) + c15 - hd * 20, e1 = e0 + 16, e2 = e0 + 32;
      ST128(__builtin_bit_cast(u32x4, c0), wsr, (i > 0 && e0 >= 0 && e0 < 20) ? dst + e0 * 80 : oob, so);
      ST128(__builtin_bit_cast(u32x4, c1), wsr, (e1 >= 0 && e1 < 20) ? dst + e1 * 80 : oob, so);
      ST128(__builtin_bit_cast(u32x4, c2), wsr, (i < 4 && e2 >= 0 && e2 < 20) ? dst + e2 * 80 : oob, so);
      ST128(__builtin_bit_cast(u32x4, cz), wsr, c15 == 0 ? dst + 1600 : oob, so);
    }
    WAIT_L0(); BAR();
    if (wave == 7) { dma_p(78); dma_p(79); }
  }
  STAMP(3);
  arrive(smem, L_CNT, 8, ws, WS_F1 + (rb * 32 + c) * 128, tag);
  STAMP(4);
}

DI void spare(char* smem, const u16* x, int sp, int rb, int c, int cb2, char* ws, u64& tag SP_ARG) {
  const int lane = threadIdx.x & 63;
  const int wave = __builtin_amdgcn_readfirstlane(threadIdx.x >> 6);
  const int g = lane >> 4, c15 = lane & 15;
  const rsrc_t wsr = __builtin_amdgcn_make_buffer_rsrc(ws, 0, WS_END, 0x00020000);
  if (threadIdx.x < 16) ((LAS u32*)(smem + L_CNT))[threadIdx.x] = 0;
  u32x4 xv[4][5];
  const int row0 = sp * 128 + wave * 16;
#pragma unroll
  for (int p = 0; p < 4; p++)
#pragma unroll
    for (int i = 0; i < 5; i++) xv[p][i] = *(const u32x4*)(x + (row0 + 4 * p + g) * D_ + (c15 + 16 * i) * 8);
  SCHED();
  tag = get_tag(ws);
#pragma unroll
  for (int p = 0; p < 4; p++) {
    float s = 0;
#pragma unroll
    for (int i = 0; i < 5; i++)
#pragma unroll
      for (int j = 0; j < 4; j++) s += blo(xv[p][i][j]) + bhi(xv[p][i][j]);
#pragma unroll
    for (int o = 8; o; o >>= 1) s += __shfl_xor(s, o);
    const float m = s * (1.f / D_);
    float q = 0;
#pragma unroll
    for (int i = 0; i < 5; i++)
#pragma unroll
      for (int j = 0; j < 4; j++) { const float a = blo(xv[p][i][j]) - m, b = bhi(xv[p][i][j]) - m; q += a * a + b * b; }
#pragma unroll
    for (int o = 8; o; o >>= 1) q += __shfl_xor(q, o);
    const float sd = rbf(sqrtf(rbf(rbf(q * (1.f / D_)) + 1e-5f)));
    ST64((u32x2{__float_as_uint(rbf(m)), __float_as_uint(1.f / sd)}), wsr, c15 == 0 ? (row0 + 4 * p + g) * 8 : 0x40000000, WS_STAT);
  }
  WAIT_V(0); WAIT_L0(); BAR();
  STAMP(1);
  if (wave == 0) {
    const u32 fo = WS_FP + (rb * 32 + (c - 30) * 15 + (c15 < 15 ? c15 : 14)) * 128;
    flag_wait(ws, fo, tag, flag_ld(ws, fo));
    if (lane == 0) flag_st(ws, WS_F1 + (rb * 32 + c) * 128, tag);
  }
  STAMP(2);
  const u32 spf = WS_F1 + ((lane & 7) * 32 + 30 + ((lane >> 3) & 1)) * 128;
  flag_wait(ws, spf, tag, flag_ld(ws, spf));
  const u32x4 srdp = mk_srd(ws, WS_END);
  const u32 lds0 = (u32)(size_t)(LAS char*)smem, l16 = lane * 16;
  asm volatile("s_nop 4" ::: "memory");
#pragma unroll
  for (int i = 0; i < 10; i++) DMA16C(lds0 + L_P + (wave * 10 + i) * 1024, l16, srdp, p_src(cb2, wave * 10 + i));
  WAIT_V(0);
  if (lane == 0) __hip_atomic_fetch_add((LAS u32*)(smem + L_CNT), 1u, __ATOMIC_RELAXED, __HIP_MEMORY_SCOPE_WORKGROUP);
  STAMP(4);
}

DI void phase2(char* smem, const u16* x, u16* out, int rb, int cb, char* ws, u64 tag SP_ARG) {
  const int lane = threadIdx.x & 63;
  const int wave = __builtin_amdgcn_readfirstlane(threadIdx.x >> 6);
  const int g = lane >> 4, c15 = lane & 15;
  const u32 l16 = lane * 16;
  const rsrc_t wsr = __builtin_amdgcn_make_buffer_rsrc(ws, 0, WS_END, 0x00020000);
  if (wave < 4) {
    const int k0 = d_k0(wave);
    const int ha = WS_HF + (rb * 54 + k0) * 4096, wa = WS_WDF + (cb * 54 + k0) * 5120;
    const u32 oob = wave < 2 ? 0 : 0x40000000;
    {
      const u32x4 srdp = mk_srd(ws, WS_END);
      const u32 m = (u32)(size_t)(LAS char*)smem + L_Q + wave * 15360;
      asm volatile("s_nop 4" ::: "memory");
#pragma unroll
      for (int i = 0; i < 15; i++) DMA16C(m + i * 1024, l16, srdp, wa + 8 * 5120 + i * 1024);
    }
    u32x4 a[14][4], b[14][5];
    auto lda = [&](int s) {
#pragma unroll
      for (int t = 0; t < 4; t++) a[s][t] = LD128(wsr, (s == 13 ? l16 + oob : l16) + t * 1024, ha + s * 4096, SC1);
    };
    auto ldb = [&](int s) {
#pragma unroll
      for (int n = 0; n < 5; n++) b[s][n] = LD128(wsr, (s == 13 ? l16 + oob : l16) + (n & 3) * 1024, wa + s * 5120 + (n >> 2) * 4096, SC1);
    };
    auto ldl = [&](int s) {
      LAS char* p = (LAS char*)smem + (s < 8 ? L_P + wave * 20480 + (s - 4) * 5120 : L_Q + wave * 15360 + (s - 8) * 5120) + l16;
#pragma unroll
      for (int n = 0; n < 5; n++) b[s][n] = *(LAS u32x4*)(p + n * 1024);
    };
#pragma unroll
    for (int s = 0; s < 4; s++) ldb(s);
    SCHED();
    {
      const int cmin = k0 / 3, cmax = (k0 + 13) / 3 < 17 ? (k0 + 13) / 3 : 17;
      const int cc = cmin + (lane & 7) < cmax ? cmin + (lane & 7) : cmax;
      const u32 fo = WS_F1 + ((rb >> 2) * 32 + cc) * 128;
      flag_wait(ws, fo, tag, flag_ld(ws, fo));
    }
    STAMP(8);
    SCHED();
#pragma unroll
    for (int s = 0; s < 5; s++) lda(s);
    lds_wait(smem, L_CNT, 8);
    f32x4 acc[4][5];
    u32x2 xr[5];
    u32 xo = 0;
#pragma unroll
    for (int s = 0; s < 14; s++) {
      SCHED();
#pragma unroll
      for (int t = 0; t < 4; t++)
#pragma unroll
        for (int n = 0; n < 5; n++) acc[t][n] = MFMA16(b[s][n], a[s][t], (s == 0 ? f32x4{0, 0, 0, 0} : acc[t][n]));
      SCHED();
      if (s == 0) STAMP(9);
      if (s == 0) lda(5);
      if (s == 1) lda(6);
      if (s == 2) { lda(7); }
      if (s == 3) { lda(8); lda(9); }
      if (s == 4) lda(10);
      if (s == 5) lda(11);
      if (s == 6) ldb(11);
      if (s == 7) lda(12);
      if (s == 8) ldb(12);
      if (s == 9) { lda(13); ldb(13); }
      if (s >= 2 && s <= 8) ldl(s + 2);
      if (s == 6) { WAIT_L0(); ((volatile LAS u32*)(smem + L_CNT))[4 + wave] = 1; }
      if (s == 10) {
        u32 l = l16;
        asm volatile("" : "+v"(l));
        xo = (rb * 64 + wave * 16 + ((l >> 4) & 15)) * D_ + cb * 80 + (l >> 8) * 4;
#pragma unroll
        for (int n = 0; n < 5; n++) xr[n] = *(const u32x2*)(x + xo + n * 16);
      }
    }
    STAMP(10);
#pragma unroll
    for (int t = 0; t < 4; t++)
      if (t != wave) {
        const int slot = t < wave ? t : t - 1;
#pragma unroll
        for (int n = 0; n < 5; n++) *(LAS f32x4*)(smem + L_Q + ((wave * 3 + slot) * 5 + n) * 1024 + l16) = acc[t][n];
      }
    f32x4 own[5];
#pragma unroll
    for (int t = 0; t < 4; t++)
      if (t == wave) {
#pragma unroll
        for (int n = 0; n < 5; n++) own[n] = acc[t][n];
      }
    WAIT_L0(); BAR();
    STAMP(11);
#pragma unroll
    for (int n = 0; n < 5; n++) {
      f32x4 bsum = own[n], ssum = {0, 0, 0, 0};
#pragma unroll
      for (int w = 0; w < 3; w++) {
        const int src = w < wave ? w : w + 1, slot = wave < src ? wave : wave - 1;
        bsum += *(LAS f32x4*)(smem + L_Q + ((src * 3 + slot) * 5 + n) * 1024 + l16);
      }
#pragma unroll
      for (int w = 0; w < 4; w++) ssum += *(LAS f32x4*)(smem + L_P + ((w * 4 + wave) * 5 + n) * 1024 + l16);
      float r[4];
#pragma unroll
      for (int j = 0; j < 4; j++) {
        const float v = bsum[j], gt = v * __builtin_amdgcn_rcpf(1.f + __builtin_amdgcn_exp2f(-1.44269504089f * v));
        const float xv = (j & 1) ? bhi(xr[n][j >> 1]) : blo(xr[n][j >> 1]);
        r[j] = xv + ssum[j] * gt;
      }
      *(u32x2*)(out + xo + n * 16) = u32x2{pk2(r[0], r[1]), pk2(r[2], r[3])};
    }
  } else {
    const int w = wave - 4;
    u32x4 pq[3], wf[5][5];
    const int wa = WS_WOF + (cb * 20 + w * 5) * 5120;
#pragma unroll
    for (int s = 0; s < 5; s++)
#pragma unroll
      for (int n = 0; n < 5; n++) wf[s][n] = LD128(wsr, l16 + (n & 3) * 1024, wa + s * 5120 + (n >> 2) * 4096, SC1);
    SCHED();
    {
      const u32 fo = WS_F1 + ((lane & 8) ? (rb >> 2) * 32 + 26 + (cb >> 1) : (lane & 7) * 32 + 18 + cb) * 128;
      flag_wait(ws, fo, tag, flag_ld(ws, fo));
    }
    STAMP(8);
    f32x4 kp[2][8];
    const int q0 = w * 64 + lane;
#pragma unroll
    for (int h = 0; h < 2; h++)
#pragma unroll
      for (int p = 0; p < 8; p++)
        kp[h][p] = __builtin_bit_cast(f32x4, LD128(wsr, (h == 0 || q0 < 164) ? q0 * 16 + h * 4096 : 0x40000000, WS_KVP + (cb * 8 + p) * 6720, SC1));
    const int qa = WS_QF + ((rb * 4 + w) * 8 + cb) * 2560;
#pragma unroll
    for (int s = 0; s < 2; s++) pq[s] = LD128(wsr, l16 + s * 1024, qa, SC1);
    { const u32x2 t = LD64(wsr, lane * 8 + 2048, qa, SC1); pq[2] = u32x4{t[0], t[1], 0, 0}; }
    SCHED();
#pragma unroll
    for (int h = 0; h < 2; h++) {
      f32x4 s = kp[h][0];
#pragma unroll
      for (int p = 1; p < 8; p++) s += kp[h][p];
      *(LAS u32x2*)(smem + L_KV + (q0 + h * 256) * 8) = u32x2{pk2(s[0], s[1]), pk2(s[2], s[3])};
    }
    WAIT_L0();
    if (lane == 0) __hip_atomic_fetch_add((LAS u32*)(smem + L_CNT + 4), 1u, __ATOMIC_RELAXED, __HIP_MEMORY_SCOPE_WORKGROUP);
    lds_wait(smem, L_CNT + 4, 4);
    STAMP(9);
    SCHED();
    f32x4 on[5], dn = {0, 0, 0, 0};
#pragma unroll
    for (int j = 0; j < 5; j++) on[j] = dn;
    auto kvfrag = [&](int hd, int row, int s) {
      const int d0 = 32 * s + 4 * g - 20 * hd, d1 = d0 + 16;
      const int base = hd * 840 + row * 40;
      const u32x2 lo = *(LAS u32x2*)(smem + L_KV + ((d0 >= 0 && d0 < 20) ? base + d0 * 2 : 3360));
      const u32x2 hi = *(LAS u32x2*)(smem + L_KV + ((d1 >= 0 && d1 < 20) ? base + d1 * 2 : 3360));
      return u32x4{lo[0], lo[1], hi[0], hi[1]};
    };
#pragma unroll
    for (int s = 0; s < 3; s++) {
#pragma unroll
      for (int j = 0; j < 5; j++) {
        constexpr int lo_[5] = {0, 0, 20, 40, 60}, hi_[5] = {20, 40, 60, 80, 80};
        if (32 * s >= hi_[j] || 32 * s + 32 <= lo_[j]) continue;
        const int e = 16 * j + c15, hd = e / 20;
        on[j] = MFMA16(kvfrag(hd, e - hd * 20, s), pq[s], on[j]);
      }
      dn = MFMA16(kvfrag(c15 & 3, 20, s), pq[s], dn);
    }
    float rd[4];
#pragma unroll
    for (int j = 0; j < 4; j++) rd[j] = __builtin_amdgcn_rcpf(dn[j] + 1e-6f);
    const float rs[5] = {rd[0], g == 0 ? rd[0] : rd[1], g < 2 ? rd[1] : rd[2], g < 3 ? rd[2] : rd[3], rd[3]};
    u32 op[5][2];
#pragma unroll
    for (int j = 0; j < 5; j++) { op[j][0] = pk2(on[j][0] * rs[j], on[j][1] * rs[j]); op[j][1] = pk2(on[j][2] * rs[j], on[j][3] * rs[j]); }
    const int oa = WS_OF + ((rb * 8 + cb) * 4 + w) * 2560;
    ST128((u32x4{op[0][0], op[0][1], op[1][0], op[1][1]}), wsr, l16, oa);
    ST128((u32x4{op[2][0], op[2][1], op[3][0], op[3][1]}), wsr, l16 + 1024, oa);
    ST64((u32x2{op[4][0], op[4][1]}), wsr, lane * 8 + 2048, oa);
    arrive(smem, L_CNT + 8, 4, ws, WS_FO + (rb * 8 + cb) * 128, tag);
    STAMP(10);
    {
      const u32 fo = WS_FO + (rb * 8 + 2 * w + (lane & 1)) * 128;
      flag_wait(ws, fo, tag, flag_ld(ws, fo));
    }
    STAMP(11);
    const int ob = WS_OF + (rb * 8 + 2 * w) * 4 * 2560;
    u32x4 of[4][5];
#pragma unroll
    for (int t = 0; t < 4; t++) {
      const int o = ob + t * 2560;
#pragma unroll
      for (int s = 0; s < 4; s++) of[t][s] = LD128(wsr, l16 + (s & 1) * 1024, o + (s >> 1) * 10240, SC1);
      const u32x2 lo = LD64(wsr, lane * 8 + 2048, o, SC1), hi = LD64(wsr, lane * 8 + 2048, o + 10240, SC1);
      of[t][4] = u32x4{lo[0], lo[1], hi[0], hi[1]};
    }
    SCHED();
    lds_wait(smem, L_CNT + 16 + 4 * w, 1);
#pragma unroll
    for (int t = 0; t < 4; t++) {
      f32x4 acc[5];
#pragma unroll
      for (int s = 0; s < 5; s++)
#pragma unroll
        for (int n = 0; n < 5; n++) acc[n] = MFMA16(wf[s][n], of[t][s], (s == 0 ? f32x4{0, 0, 0, 0} : acc[n]));
#pragma unroll
      for (int n = 0; n < 5; n++) *(LAS f32x4*)(smem + L_P + ((w * 4 + t) * 5 + n) * 1024 + l16) = acc[n];
    }
    STAMP(12);
    WAIT_L0(); BAR();
    if (blockIdx.x == 0 && wave == 7) {
#pragma unroll 1
      for (int i = 0; i < 4; i++) { const u32 fo = WS_FO + (i * 64 + lane) * 128; flag_wait(ws, fo, tag, flag_ld(ws, fo)); }
      if (lane == 0) flag_st(ws, WS_EPOCH, tag);
    }
  }
}

__global__ __launch_bounds__(512, 2) void fused(const u16* __restrict__ x, const u16* __restrict__ wqkv, const u16* __restrict__ wo,
                                                const u16* __restrict__ wg, const u16* __restrict__ wu, const u16* __restrict__ wd,
                                                u16* __restrict__ out, char* ws) {
  extern __shared__ __attribute__((aligned(16))) char smem[];
  STAMP_INIT(); STAMP(0);
  const int xcd = blockIdx.x & 7, idx = blockIdx.x >> 3;
  const int rb = (xcd >> 2) * 4 + (idx & 3), c = (idx >> 2) * 4 + (xcd & 3);
  const int rb2 = xcd * 4 + (idx >> 3), cb2 = idx & 7;
  u64 tag;
  if (c >= 30) spare(smem, x, (c - 30) * 8 + rb, rb, c, cb2, ws, tag SP_PASS);
  else if (c < 18) phase1<0>(smem, x, wd, wo, wg + c * 96, wu + c * 96, (D_ * F_ - c * 96) * 2, (D_ * F_ - c * 96) * 2, F_ * 2, 0, 0, rb, c, c, cb2, ws, tag SP_PASS);
  else if (c < 26) {
    const int hg = c - 18, o = 80 * hg - 16 * (hg & 1);
    phase1<1>(smem, x, wd, wo, wqkv + 640 + o, wqkv + 1280 + o, (D_ * 1920 - 640 - o) * 2, (D_ * 1920 - 1280 - o) * 2, 3840, hg & 1, hg & 1, rb, hg, c, cb2, ws, tag SP_PASS);
  } else {
    const int qb = c - 26;
    phase1<2>(smem, x, wd, wo, wqkv + 160 * qb, wqkv + 160 * qb + 64, (D_ * 1920 - 160 * qb) * 2, (D_ * 1920 - 160 * qb - 64) * 2, 3840, 0, 1, rb, qb, c, cb2, ws, tag SP_PASS);
  }
  phase2(smem, x, out, rb2, cb2, ws, tag SP_PASS);
  STAMP(15); STAMP_FLUSH(ws);
}

extern "C" void kernel_launch(void* const* d_in, const int* in_sizes, int n_in, void* d_out, int out_size, void* d_ws, size_t ws_size,
                              hipStream_t stream) {
  (void)hipFuncSetAttribute((const void*)fused, hipFuncAttributeMaxDynamicSharedMemorySize, LDS_BYTES);
  fused<<<256, 512, LDS_BYTES, stream>>>((const u16*)d_in[0], (const u16*)d_in[1], (const u16*)d_in[2], (const u16*)d_in[4],
                                         (const u16*)d_in[5], (const u16*)d_in[6], (u16*)d_out, (char*)d_ws);
}
```

```cpp
#include <hip/hip_runtime.h>
#include <type_traits>
#define LATE (c >= 18 && c < 26)
#define EPRIO 1
#define DP_RUN 0
#define SP_W 2
#define SP_HI 2
#define SP_POLL 2
#define CODEWARM 46000
typedef unsigned short u16;
typedef unsigned int u32;
typedef unsigned long long u64;
typedef short s16x4 __attribute__((ext_vector_type(4)));
typedef short s16x8 __attribute__((ext_vector_type(8)));
typedef __bf16 bf16x8 __attribute__((ext_vector_type(8)));
typedef __bf16 bf16x2 __attribute__((ext_vector_type(2)));
typedef float f32x2 __attribute__((ext_vector_type(2)));
typedef float f32x4 __attribute__((ext_vector_type(4)));
typedef u32 u32x2 __attribute__((ext_vector_type(2)));
typedef u32 u32x4 __attribute__((ext_vector_type(4)));
#define LAS __attribute__((address_space(3)))
#define GAS __attribute__((address_space(1)))
#define DI __device__ __forceinline__

constexpr int S_ = 2048, D_ = 640, F_ = 1728;
constexpr int WS_EPOCH = 0;
constexpr int WS_FG   = 4096;
constexpr int WS_FKV  = WS_FG + 8192;
constexpr int WS_FP   = WS_FKV + 1024;
constexpr int WS_FK   = 24576;
constexpr int WS_STAT = 16384;
constexpr int WS_KVC  = 32768;
constexpr int WS_KVP  = 65536;
constexpr int WS_HF   = 1 << 20;
constexpr int WS_QF   = WS_HF + 32 * 54 * 4096;
constexpr int WS_WDF  = WS_QF + 128 * 20 * 1024;
constexpr int WS_WOF  = WS_WDF + 8 * 54 * 5120;
constexpr int WS_END  = WS_WOF + 8 * 20 * 5120;
constexpr size_t WS_DBG = 64u << 20;
constexpr u64 TAG_STEP = 0x9E3779B97F4A7C15ull;
constexpr u32 SPIN_CAP = 1u << 16;
constexpr int SC1 = 16;
#define LDAUX 16
#define LWAUX 16
constexpr int OOB = 0x40000000;

constexpr int L_X0 = 0, L_W1 = 32768, L_W2 = 57344, L_X1 = 81920, L_W0 = 114688, L_SP = 139264;
constexpr int W_IMG1 = 12288;
constexpr int L_R = 81920, R_SZ = 19968;
constexpr int L_ST = 161792;
constexpr int L_CS = 162816;
constexpr int L_CNT = 163584;
constexpr int LDS_BYTES = 163840;
DI constexpr int p_off(int f) { return f < 8 ? L_X0 + f * 1024 : f < 14 ? L_W1 + (f - 8) * 1024 : L_W2 + (f - 14) * 1024; }
DI int p_base(int w, int f) { return w * (f < 8 ? 8192 : 6144); }

DI u32 pk2(float a, float b) { f32x2 v = {a, b}; return __builtin_bit_cast(u32, __builtin_convertvector(v, bf16x2)); }
DI float blo(u32 d) { return __uint_as_float(d << 16); }
DI float bhi(u32 d) { return __uint_as_float(d & 0xffff0000u); }
DI float rbf(float f) { return blo(pk2(f, f)); }
DI u32x4 mk_srd(const void* p, u32 bytes) {
  u64 a = (u64)p;
  u32x4 r;
  r[0] = __builtin_amdgcn_readfirstlane((u32)a); r[1] = __builtin_amdgcn_readfirstlane((u32)(a >> 32) & 0xffffu);
  r[2] = __builtin_amdgcn_readfirstlane(bytes); r[3] = 0x00020000u;
  return r;
}
#define DMA16(m0v, voff, srd, soff) \
  asm volatile("s_mov_b32 m0, %0\n\ts_nop 0\n\tbuffer_load_dwordx4 %1, %2, %3 offen lds" ::"s"(m0v), "v"(voff), "s"(srd), "s"(soff) : "memory")
#define DMA16C(m0v, voff, srd, soff) \
  asm volatile("s_mov_b32 m0, %0\n\ts_nop 0\n\tbuffer_load_dwordx4 %1, %2, %3 offen sc1 lds" ::"s"(m0v), "v"(voff), "s"(srd), "s"(soff) : "memory")
#define TOUCH(m0v, voff, srd, soff) \
  asm volatile("s_mov_b32 m0, %0\n\ts_nop 0\n\tbuffer_load_dword %1, %2, %3 offen lds" ::"s"(m0v), "v"(voff), "s"(srd), "s"(soff) : "memory")
#define WAIT_V(n) asm volatile("s_waitcnt vmcnt(" #n ")" ::: "memory")
#define WAIT_L0() do { asm volatile("" ::: "memory"); __builtin_amdgcn_s_waitcnt(0xC07F); asm volatile("" ::: "memory"); } while (0)
#define BAR() __builtin_amdgcn_s_barrier()
#define SCHED() __builtin_amdgcn_sched_barrier(0)
#define MFMA16(a, b, c) __builtin_amdgcn_mfma_f32_16x16x32_bf16(__builtin_bit_cast(bf16x8, a), __builtin_bit_cast(bf16x8, b), c, 0, 0, 0)
#define LD128(r, v, s, aux) __builtin_amdgcn_raw_buffer_load_b128(r, v, s, aux)
#define LD64(r, v, s, aux) __builtin_amdgcn_raw_buffer_load_b64(r, v, s, aux)
#define ST128(x, r, v, s) __builtin_amdgcn_raw_buffer_store_b128(x, r, (v) + (s), 0, SC1)
#define ST64(x, r, v, s) __builtin_amdgcn_raw_buffer_store_b64(x, r, (v) + (s), 0, SC1)
typedef __amdgpu_buffer_rsrc_t rsrc_t;

#define STAMP_INIT() do {} while (0)
#define STAMP(i) do {} while (0)
#define IVSTAMP(t) do {} while (0)
#define STAMP_FLUSH(ws) do {} while (0)
#define SP_ARG
#define SP_PASS

DI u64 flag_ld(char* ws, u32 off) { return __hip_atomic_load((GAS u64*)(ws + off), __ATOMIC_RELAXED, __HIP_MEMORY_SCOPE_AGENT); }
DI void flag_st(char* ws, u32 off, u64 v) { __hip_atomic_store((GAS u64*)(ws + off), v, __ATOMIC_RELAXED, __HIP_MEMORY_SCOPE_AGENT); }
template <int SLEEP = 2>
DI void flag_wait(char* ws, u32 off, u64 tag, u64 v) {
#pragma unroll 1
  for (u32 spins = 0; !__all(v == tag) && spins < SPIN_CAP; spins++) {
    __builtin_amdgcn_s_sleep(SLEEP);
    v = flag_ld(ws, off);
  }
  __builtin_amdgcn_fence(__ATOMIC_ACQUIRE, "wavefront");
}
DI void lds_wait(char* smem, int off, u32 want) {
#pragma unroll 1
  for (u32 spins = 0; spins < SPIN_CAP; spins++) {
    const u32 v = __hip_atomic_load((LAS u32*)(smem + off), __ATOMIC_RELAXED, __HIP_MEMORY_SCOPE_WORKGROUP);
    if (__builtin_amdgcn_readfirstlane(v) >= want) break;
    __builtin_amdgcn_s_sleep(1);
  }
  asm volatile("" ::: "memory");
}
#define PSLEEP 6
template <int SLEEP = PSLEEP>
DI void group_wait(char* smem, int word, bool leader, char* ws, u32 off, u64 tag) {
  if (leader) {
    flag_wait<SLEEP>(ws, off, tag, flag_ld(ws, off));
    *(volatile LAS u32*)(smem + word) = 1;
  } else lds_wait(smem, word, 1);
}
DI void arrive(char* smem, int cnt, u32 n, char* ws, u32 flag, u64 tag, int rep = 1, int stride = 0) {
  WAIT_V(0);
  const int lane = threadIdx.x & 63;
  u32 k = 0;
  if (lane == 0) k = __hip_atomic_fetch_add((LAS u32*)(smem + cnt), 1u, __ATOMIC_RELAXED, __HIP_MEMORY_SCOPE_WORKGROUP);
  if (__builtin_amdgcn_readfirstlane(k) == n - 1 && lane < rep) flag_st(ws, flag + lane * stride, tag);
}
DI u64 get_tag(char* ws) {
  u64 e;
  asm volatile("s_load_dwordx2 %0, %1, 0x0\n\ts_waitcnt lgkmcnt(0)" : "=s"(e) : "s"(ws + WS_EPOCH) : "memory");
  return e + TAG_STEP;
}
DI int d_k0(int w) { return w < 2 ? w * 14 : 2 + w * 13; }
DI int d_rot() {
  const int idx = blockIdx.x >> 3;
  return (((idx & 7) + (idx >> 3)) % 3) * 5;
}
DI int d_step(int s, int r) { return s + r >= 14 ? s + r - 14 : s + r; }
struct P1 {
  u32x4 srd;
  u32 vo[6];
  int pitch, unit;
};

template <int TYPE>
DI void phase1(char* smem, P1& p, int o0, int o1, int rb, int cb, int c, int cb2, char* ws, u64 tag SP_ARG) {
  constexpr int NT = TYPE == 0 ? 6 : 5;
  const int lane = threadIdx.x & 63;
  const int wave = __builtin_amdgcn_readfirstlane(threadIdx.x >> 6);
  const int grp = wave >> 2, wg = wave & 3, wm = wave >> 1, wn = wave & 1;
  const u32 lds0 = (u32)(size_t)(LAS char*)smem;
  const u32 mb = lds0 + wg * (grp == 0 ? 8192 : 6144);
  const int g = lane >> 4, c15 = lane & 15;
  const u32 l16 = lane * 16;
  constexpr int NB = TYPE == 1 ? 5 : TYPE == 0 ? 3 : 2;
  u32 ax[2], aw[NB];
  const int ksw = wm & 1;
  ax[0] = lds0 + (wm * 64 + c15) * 128 + (((4 * ksw + g) ^ (c15 >> 1)) << 4);
  ax[1] = ax[0] ^ 64;
  {
    const u32 awr = lds0 + (8 * g + (c15 >> 2)) * 192, sw = (g & 1) << 5, p8 = (lane & 3) * 8;
#pragma unroll
    for (int n = 0; n < NB; n++) {
      if (TYPE == 0) aw[n] = awr + (((n < 2 ? 4 * wn + n : 2 + wn) * 32 + p8) ^ sw);
      else if (TYPE == 2) aw[n] = awr + wn * W_IMG1 + ((((wn ? o1 : o0) + n) * 32 + p8) ^ sw);
      else {
        const int j = 32 * n + p8;
        aw[n] = awr + (j < 80 ? 0 : W_IMG1) + ((32 * o0 + 80 * wn + (j < 80 ? j : j - 80)) ^ sw);
      }
    }
  }
  float cs[NT];
#pragma unroll
  for (int n = 0; n < NT; n++) cs[n] = 0.f;
  f32x4 acc[4][NT];
#pragma unroll
  for (int m = 0; m < 4; m++)
#pragma unroll
    for (int n = 0; n < NT; n++) acc[m][n] = f32x4{0, 0, 0, 0};
  s16x8 xf[2][4], wf[2][NT];
  const rsrc_t wsr = __builtin_amdgcn_make_buffer_rsrc(ws, 0, WS_END, 0x00020000);
  const u32x4 srdp = mk_srd(ws, WS_END);
  const u32 psrc = WS_WDF + (cb2 * 54 + d_k0(wg) + d_step(4, d_rot())) * 5120;
  u32x4 srd = p.srd;
  u32 xstep = 16 * D_ * 2;

  auto trd = [&](LAS char* a) { return __builtin_amdgcn_ds_read_tr16_b64_v4i16((LAS s16x4*)a); };
  auto dma_x = [&](int i, u32 m, u32 so) { DMA16C(m + i * 1024, p.vo[i & 1], srd, so + (i >> 1) * xstep); };
  auto dma_w = [&](int i, u32 m, u32 so) { DMA16C(m + i * 1024, p.vo[i], srd, so); };
  auto load_phase = [&](auto mode, u32 xo, u32 wo, u32 m, u32 so) {
    constexpr int MODE = decltype(mode)::value;
#pragma unroll
    for (int k2 = 0; k2 < 2; k2++) {
      const u32 wk = wo + (k2 ^ ksw) * (32 * 192);
      u32 bw[NB], bx = ax[k2] + xo;
#pragma unroll
      for (int n = 0; n < NB; n++) { bw[n] = aw[n] + wk; asm volatile("" : "+v"(bw[n])); }
      asm volatile("" : "+v"(bx));
      if (MODE == 1) dma_x(4 * k2, m, so);
#pragma unroll
      for (int mm = 0; mm < 4; mm++) xf[k2][mm] = *(LAS s16x8*)((LAS char*)(size_t)bx + mm * 2048);
#pragma unroll
      for (int n = 0; n < NT; n++) {
        if (!(n & 1)) { if (MODE == 1) dma_x(4 * k2 + 1 + n / 2, m, so); if (MODE == 2) dma_w(3 * k2 + n / 2, m, so); }
        LAS char* a = (LAS char*)(size_t)bw[n % NB] + (TYPE == 0 ? (n / 3) * W_IMG1 : TYPE == 2 ? (n / 2) * 64 : 0);
        s16x4 lo = trd(a), hi = trd(a + 4 * 192);
        wf[k2][n] = __builtin_shufflevector(lo, hi, 0, 1, 2, 3, 4, 5, 6, 7);
      }
    }
  };
  auto compute = [&]() {
#pragma unroll
    for (int k2 = 0; k2 < 2; k2++)
#pragma unroll
      for (int m = 0; m < 4; m++)
#pragma unroll
        for (int n = 0; n < NT; n++)
          acc[m][n] = TYPE == 1 ? MFMA16(xf[k2][m], wf[k2][n], acc[m][n]) : MFMA16(wf[k2][n], xf[k2][m], acc[m][n]);
  };
#define CSMODE 2
#define CS_ALLA
  constexpr int CSA = NT;
  f32x4 cm[NT];
#pragma unroll
  for (int n = 0; n < NT; n++) cm[n] = f32x4{0, 0, 0, 0};
  auto colsum = [&](int n0, int n1) {
#pragma unroll
    for (int n = n0; n < n1; n++) {
      const u32x4 d = __builtin_bit_cast(u32x4, wf[0][n]);
      if (CSMODE == 0) {
        float s0 = blo(d[0]) + bhi(d[0]), s1 = blo(d[1]) + bhi(d[1]), s2 = blo(d[2]) + bhi(d[2]), s3 = blo(d[3]) + bhi(d[3]);
        cs[n] += (s0 + s1) + (s2 + s3);
      } else if (CSMODE == 1) {
        const bf16x2 one = {(__bf16)1.f, (__bf16)1.f};
#pragma unroll
        for (int j = 0; j < 4; j++) cs[n] = __builtin_amdgcn_fdot2_f32_bf16(__builtin_bit_cast(bf16x2, d[j]), one, cs[n], false);
      } else {
        const u32x4 ones = {0x3f803f80u, 0x3f803f80u, 0x3f803f80u, 0x3f803f80u};
        cm[n] = MFMA16(ones, d, cm[n]);
      }
    }
  };
  auto interleave = [&]() {
    if (CSMODE == 2) return;
#pragma unroll
    for (int i = 0; i < 8 * NT; i++) { __builtin_amdgcn_sched_group_barrier(0x8, CSMODE == 1 ? 3 : 1, 0); __builtin_amdgcn_sched_group_barrier(0x2, CSA == NT && CSMODE == 0 ? 2 : 1, 0); }
  };
  auto cs_out = [&](int n0, int n1) {
#pragma unroll
    for (int n = n0; n < n1; n++) {
      if (CSMODE == 2) {
        if (lane < 16) __hip_atomic_fetch_add((LAS float*)(smem + L_CS) + (wn * 6 + n) * 16 + lane, cm[n][0], __ATOMIC_RELAXED, __HIP_MEMORY_SCOPE_WORKGROUP);
        continue;
      }
      const auto a = __builtin_amdgcn_permlane32_swap(__float_as_uint(cs[n]), __float_as_uint(cs[n]), false, false);
      const float s = __uint_as_float(a[0]) + __uint_as_float(a[1]);
      const auto b = __builtin_amdgcn_permlane16_swap(__float_as_uint(s), __float_as_uint(s), false, false);
      const float r = __uint_as_float(b[0]) + __uint_as_float(b[1]);
      if (lane < 16) __hip_atomic_fetch_add((LAS float*)(smem + L_CS) + (wn * 6 + n) * 16 + lane, r, __ATOMIC_RELAXED, __HIP_MEMORY_SCOPE_WORKGROUP);
    }
  };
  auto wait_tile0 = [&]() {
    const u32 t32 = (u32)tag;
#pragma unroll 1
    for (u32 spins = 0; spins < (SPIN_CAP << 4); spins++) {
      const u32x4 v = *(volatile LAS u32x4*)(smem + L_CNT + 64);
      if (__all(v[0] == t32 && v[1] == t32 && v[2] == t32 && v[3] == t32)) break;
      __builtin_amdgcn_s_sleep(1);
    }
    asm volatile("" ::: "memory");
  };
  using I0 = std::integral_constant<int, 0>; using I1 = std::integral_constant<int, 1>; using I2 = std::integral_constant<int, 2>;
  asm volatile("" : "+v"(ax[0]), "+v"(ax[1]));
#pragma unroll
  for (int n = 0; n < NB; n++) asm volatile("" : "+v"(aw[n]));
  if (grp == 0) {
    u32 ua, ub, ud[4];
    {
      const int uu = p.unit < 740 ? p.unit : 739, uks = uu / 10, ucg = uu - uks * 10;
      const bool alt = uks < 54 && uks % 3 == 1;
      ua = lds0 + L_SP + wg * 4096 + ((alt ? 16 * (g >> 1) + 4 * (g & 1) : 4 * g) + (c15 >> 2)) * 128 + (lane & 3) * 8;
      ub = alt ? 8 * 128 : 16 * 128;
#pragma unroll
      for (int j = 0; j < 4; j++) {
        const int n16 = ucg * 4 + j, cbk = n16 / 5, nt = n16 - cbk * 5;
        ud[j] = p.unit >= 740 ? OOB : (uks < 54 ? WS_WDF + (cbk * 54 + uks) * 5120 : WS_WOF + (cbk * 20 + uks - 54) * 5120) + nt * 1024;
      }
      asm volatile("" : "+v"(ua), "+s"(ub), "+s"(ud[0]), "+s"(ud[1]), "+s"(ud[2]), "+s"(ud[3]));
    }
    WAIT_V(0);
    *(volatile LAS u32*)(smem + L_CNT + 64 + wg * 4) = (u32)tag;
    wait_tile0();
    STAMP(1);
    SCHED();
    load_phase(I0{}, L_X0, L_W0, 0, 0); SCHED(); compute(); colsum(0, CSA); interleave(); SCHED();
    BAR(); SCHED();
    STAMP(6);
    u32 w_cur = L_W1, w_nxt = L_W2, w_prv = L_W0, so = 2 * 128;
#pragma unroll 1
    for (int t = 1; t < 10; t++) {
      const u32 xo = (t & 1) * L_X1;
      if (t == 2) {
#pragma unroll
        for (int j = 0; j < 4; j++) {
          const s16x4 lo = trd((LAS char*)(size_t)ua + j * 32), hi = trd((LAS char*)(size_t)(ua + ub) + j * 32);
          ST128(__builtin_bit_cast(u32x4, __builtin_shufflevector(lo, hi, 0, 1, 2, 3, 4, 5, 6, 7)), wsr, l16, ud[j]);
        }
      }
      SCHED();
      load_phase(I1{}, xo, w_cur, mb + (L_X1 - xo), so);
      { const u32 u = w_prv; w_prv = w_cur; w_cur = w_nxt; w_nxt = u; }
      so += 128;
      SCHED(); compute(); colsum(0, CSA); interleave(); SCHED();
      if (t == 8) { srd = srdp; so = psrc; xstep = 2048; p.vo[0] = l16; p.vo[1] = l16 + 1024; }
      if (t == 3) STAMP(7);
      if (t == 7) STAMP(13);
      if (t == 8) STAMP(14);
      if (t < 9) WAIT_V(0); else { cs_out(0, CSA); WAIT_L0(); }
      if (t == 2) arrive(smem, L_CNT + 12, 4, ws, WS_FP + (c * 8 + rb) * 8, tag);
      BAR(); SCHED();
    }
  } else {
    __builtin_amdgcn_s_setprio(1);
#pragma unroll
    for (int m = 0; m < 4; m++)
#pragma unroll
      for (int n = 0; n < NT; n++) asm volatile("" : "+v"(acc[m][n]));
    wait_tile0();
    STAMP(1);
    SCHED();
    load_phase(I0{}, L_X0, L_W0, 0, 0); WAIT_L0(); WAIT_V(10); SCHED(); BAR(); SCHED();
    STAMP(6);
    u32 w_cur = L_W1, w_nxt = L_W2, w_prv = L_W0, so = 3 * 64 * p.pitch, sstep = 64 * p.pitch;
    const u32 fso = WS_FP + (wg * 64 + lane) * 8;
    u64 fv = 0;
#pragma unroll 1
    for (int t = 1; t < 10; t++) {
      compute(); colsum(CSA, NT); interleave(); SCHED();
      if (t == 7) asm volatile("global_load_dwordx2 %0, %1, %2 sc1" : "+v"(fv) : "v"(fso), "s"(ws) : "memory");
      if (t == 8) {
        srd = srdp; so = psrc + 8192; sstep = 6144;
#pragma unroll
        for (int i = 0; i < 6; i++) p.vo[i] = l16 + i * 1024;
        DMA16C(lds0 + L_ST, l16, srdp, WS_STAT + rb * 1024);
      }
      SCHED();
      load_phase(I2{}, (t & 1) * L_X1, w_cur, mb + w_prv, so);
      { const u32 u = w_prv; w_prv = w_cur; w_cur = w_nxt; w_nxt = u; }
      so += sstep;
      if (t == 9) { colsum(CSA, NT); cs_out(CSA, NT); }
      else asm volatile("s_waitcnt vmcnt(6)" : "+v"(fv) :: "memory");
      if (t == 7) { flag_wait(ws, fso, tag, fv); }
      WAIT_L0();
      SCHED(); BAR(); SCHED();
    }
    compute(); SCHED();
    __builtin_amdgcn_s_setprio(0);
  }
  STAMP(2);

  LAS float* csl = (LAS float*)(smem + L_CS) + wn * 96;
  LAS u32* stl = (LAS u32*)(smem + L_ST) + wm * 64;
  auto lo2 = [](f32x4 v) { return f32x2{v[0], v[1]}; };
  auto hi2 = [](f32x4 v) { return f32x2{v[2], v[3]}; };
  auto fma2 = [](f32x2 a, f32x2 b, f32x2 c) { return __builtin_elementwise_fma(a, b, c); };
  auto pkv = [](f32x2 v) { return __builtin_bit_cast(u32, __builtin_convertvector(v, bf16x2)); };
  auto phi2 = [&](f32x2 t, f32x2 rs, f32x2 rl, float c1 = 1.f + 1e-6f, float c2 = 1e-6f) {
    const f32x2 u = t * rl, lin = fma2(t, rs, f32x2{c1, c1});
    const f32x2 e = f32x2{__builtin_amdgcn_fmed3f(__builtin_amdgcn_exp2f(u[0]), 0.f, 1.f), __builtin_amdgcn_fmed3f(__builtin_amdgcn_exp2f(u[1]), 0.f, 1.f)} + f32x2{c2, c2};
    return f32x2{fmaxf(lin[0], e[0]), fmaxf(lin[1], e[1])};
  };
  if (TYPE != 1) {
    f32x4 cv[NT];
#pragma unroll
    for (int n = 0; n < NT; n++) cv[n] = *(LAS f32x4*)(csl + n * 16 + g * 4);
    u32 odd[4][2];
#pragma unroll
    for (int m = 0; m < 4; m++) {
      const u32 sv = stl[m * 16 + c15];
      const float mu1 = -blo(sv), r = __builtin_amdgcn_rcpf(bhi(sv));
      const f32x2 mu = {mu1, mu1};
      u32 o[5][2];
      if (TYPE == 0) {
        const f32x2 r2 = {r * r, r * r};
#pragma unroll
        for (int n = 0; n < 3; n++)
#pragma unroll
          for (int h = 0; h < 2; h++) {
            const f32x2 gt = fma2(mu, h ? hi2(cv[n]) : lo2(cv[n]), h ? hi2(acc[m][n]) : lo2(acc[m][n]));
            const f32x2 up = fma2(mu, h ? hi2(cv[(n + 3) % NT]) : lo2(cv[(n + 3) % NT]), h ? hi2(acc[m][(n + 3) % NT]) : lo2(acc[m][(n + 3) % NT]));
            o[n][h] = pkv(f32x2{fmaxf(gt[0], 0.f), fmaxf(gt[1], 0.f)} * (up * r2));
          }
        const int base = WS_HF + ((rb * 4 + wm) * 54 + cb * 3) * 4096;
        ST128((u32x4{o[0][0], o[0][1], o[1][0], o[1][1]}), wsr, l16 + m * 1024, base + wn * 8192);
        odd[m][0] = o[2][0]; odd[m][1] = o[2][1];
        if (m & 1) {
          const auto s0 = __builtin_amdgcn_permlane32_swap(odd[m - 1][0], odd[m][0], false, false);
          const auto s1 = __builtin_amdgcn_permlane32_swap(odd[m - 1][1], odd[m][1], false, false);
          ST128((u32x4{s0[0], s1[0], s0[1], s1[1]}), wsr, (lane >> 5) * 1024 + (lane & 31) * 16 + (m >> 1) * 2048, base + 4096 + wn * 512);
        }
      } else {
        const f32x2 rs = {r, r}, rl = {r * 1.44269504089f, r * 1.44269504089f};
#pragma unroll
        for (int n = 0; n < 5; n++) {
          o[n][0] = pkv(phi2(fma2(mu, lo2(cv[n]), lo2(acc[m][n])), rs, rl));
          o[n][1] = pkv(phi2(fma2(mu, hi2(cv[n]), hi2(acc[m][n])), rs, rl));
        }
        const int base = WS_QF + ((rb * 16 + wm * 4 + m) * 8 + cb * 2 + wn) * 2560;
        ST128((u32x4{o[0][0], o[0][1], o[1][0], o[1][1]}), wsr, l16, base);
        ST128((u32x4{o[2][0], o[2][1], o[3][0], o[3][1]}), wsr, l16 + 1024, base);
        ST64((u32x2{o[4][0], o[4][1]}), wsr, lane * 8 + 2048, base);
      }
    }
  } else {
    f32x2 cv[5];
#pragma unroll
    for (int n = 0; n < 5; n++) { const float v = csl[n * 16 + c15]; cv[n] = f32x2{v, v}; }
    const float mc1 = c15 < 8 ? 1.f + 1e-6f : 0.f, mc2 = c15 < 8 ? 1e-6f : -__builtin_inff();
    f32x4 kv[7], kz[3];
    const u32x4 ones = {0x3f803f80u, 0x3f803f80u, 0x3f803f80u, 0x3f803f80u};
    constexpr int ja[7] = {0, 0, 1, 1, 1, 2, 2}, jb[7] = {2, 3, 2, 3, 4, 3, 4};
#pragma unroll
    for (int mp = 0; mp < 2; mp++) {
      u32x4 fr[5];
#pragma unroll
      for (int mm = 0; mm < 2; mm++) {
        const int m = 2 * mp + mm;
        const u32x4 sv = *(LAS u32x4*)(stl + m * 16 + g * 4);
#pragma unroll
        for (int h = 0; h < 2; h++) {
          const f32x2 mu = {-blo(sv[2 * h]), -blo(sv[2 * h + 1])};
          const f32x2 rs = {__builtin_amdgcn_rcpf(bhi(sv[2 * h])), __builtin_amdgcn_rcpf(bhi(sv[2 * h + 1]))};
          const f32x2 rl = rs * f32x2{1.44269504089f, 1.44269504089f};
#pragma unroll
          for (int n = 0; n < 5; n++) {
            const f32x2 t = fma2(mu, cv[n], h ? hi2(acc[m][n]) : lo2(acc[m][n]));
            fr[n][2 * mm + h] = n < 2 ? pkv(phi2(t, rs, rl)) : n > 2 ? pkv(t * rs) : pkv(phi2(t, rs, rl, mc1, mc2));
          }
        }
      }
#pragma unroll
      for (int i = 0; i < 7; i++) kv[i] = MFMA16(fr[ja[i]], fr[jb[i]], (mp == 0 ? f32x4{0, 0, 0, 0} : kv[i]));
#pragma unroll
      for (int i = 0; i < 3; i++) kz[i] = MFMA16(fr[i], ones, (mp == 0 ? f32x4{0, 0, 0, 0} : kz[i]));
    }
    int ob[3], eb[3];
#pragma unroll
    for (int i = 0; i < 3; i++) {
      const int d = 16 * i + 4 * g, hl = d >= 20;
      ob[i] = L_R + wm * 6720 + (2 * wn + hl) * 1680 + (d - 20 * hl) * 4;
      eb[i] = d < 40 ? c15 - 40 - 20 * hl : 1000;
    }
#pragma unroll
    for (int i = 0; i < 7; i++) {
      const u32 e = eb[ja[i]] + 16 * jb[i];
      if (e < 20u) *(LAS f32x4*)(smem + ob[ja[i]] + e * 80) = kv[i];
    }
#pragma unroll
    for (int i = 0; i < 3; i++)
      if (c15 == 0 && eb[i] < 0) *(LAS f32x4*)(smem + ob[i] + 1600) = kz[i];
    WAIT_L0(); STAMP(5); BAR();
    if (threadIdx.x < 420) {
      LAS char* q = (LAS char*)smem + L_R + threadIdx.x * 16;
      const f32x4 t = (*(LAS f32x4*)q + *(LAS f32x4*)(q + 6720)) + (*(LAS f32x4*)(q + 2 * 6720) + *(LAS f32x4*)(q + 3 * 6720));
      ST128(__builtin_bit_cast(u32x4, t), wsr, threadIdx.x * 16, WS_KVP + (cb * 8 + rb) * 6720);
    }
  }
  STAMP(3);
  if (lane == 0) __hip_atomic_fetch_add((LAS u32*)(smem + L_CNT + 4), 1u, __ATOMIC_RELAXED, __HIP_MEMORY_SCOPE_WORKGROUP);
  arrive(smem, L_CNT, 8, ws, TYPE == 1 ? WS_FKV + (cb * 16 + rb) * 8 : WS_FG + (rb * 128 + c) * 8, tag, TYPE == 1 ? 1 : 4, 256);
  STAMP(4);
  lds_wait(smem, L_CNT, 8);
}

DI void spare(char* smem, const u16* x, int rb, int c, int cb2, char* ws, u64& tag SP_ARG) {
  const int lane = threadIdx.x & 63;
  const int wave = __builtin_amdgcn_readfirstlane(threadIdx.x >> 6);
  const int g = lane >> 4, c15 = lane & 15;
  const rsrc_t wsr = __builtin_amdgcn_make_buffer_rsrc(ws, 0, WS_END, 0x00020000);
  u32x4 xv[4][5];
  const int row0 = rb * 256 + (c - 30) * 128 + wave * 16;
#pragma unroll
  for (int p = 0; p < 4; p++)
#pragma unroll
    for (int i = 0; i < 5; i++) xv[p][i] = *(const u32x4*)(x + (row0 + 4 * p + g) * D_ + (c15 + 16 * i) * 8);
  SCHED();
  tag = get_tag(ws);
#pragma unroll 1
  for (int p = 0; p < 4; p++) {
    u32x4 v[5];
#pragma unroll
    for (int i = 0; i < 5; i++) { v[i] = xv[0][i]; xv[0][i] = xv[1][i]; xv[1][i] = xv[2][i]; xv[2][i] = xv[3][i]; }
    float s = 0;
#pragma unroll
    for (int i = 0; i < 5; i++)
#pragma unroll
      for (int j = 0; j < 4; j++) s += blo(v[i][j]) + bhi(v[i][j]);
#pragma unroll
    for (int o = 8; o; o >>= 1) s += __shfl_xor(s, o);
    const float m = s * (1.f / D_);
    float q = 0;
#pragma unroll
    for (int i = 0; i < 5; i++)
#pragma unroll
      for (int j = 0; j < 4; j++) { const float a = blo(v[i][j]) - m, b = bhi(v[i][j]) - m; q += a * a + b * b; }
#pragma unroll
    for (int o = 8; o; o >>= 1) q += __shfl_xor(q, o);
    const float sd = sqrtf(rbf(rbf(q * (1.f / D_)) + 1e-5f));
    __builtin_amdgcn_raw_buffer_store_b32(pk2(m, sd), wsr, c15 == 0 ? WS_STAT + (row0 + 4 * p + g) * 4 : OOB, 0, SC1);
  }
  WAIT_V(0); WAIT_L0(); BAR();
  if (threadIdx.x == 0) flag_st(ws, WS_FP + (c * 8 + rb) * 8, tag);
  STAMP(2);
  if (wave < 4) {
    const u32 fso = WS_FP + (wave * 64 + lane) * 8;
    flag_wait<16>(ws, fso, tag, flag_ld(ws, fso));
  }
  BAR();
  STAMP(13);
  if (c == 31 && rb == 0 && threadIdx.x == 0) flag_st(ws, WS_EPOCH, tag);
  if (wave < 4) {
    const u32x4 srdp = mk_srd(ws, WS_END);
    const u32 lds0 = (u32)(size_t)(LAS char*)smem, l16 = lane * 16;
    const u32 psrc = WS_WDF + (cb2 * 54 + d_k0(wave) + d_step(4, d_rot())) * 5120;
    asm volatile("s_nop 4" ::: "memory");
#pragma unroll
    for (int i = 0; i < 20; i++) DMA16C(lds0 + p_off(i) + p_base(wave, i), l16, srdp, psrc + i * 1024);
  }
  if (threadIdx.x == 0) *(volatile LAS u32*)(smem + L_CNT) = 8;
  STAMP(4);
}

DI void phase2(char* smem, const u16* x, u16* out, int rb, int cb, int red, char* ws, u64 tag SP_ARG) {
  const int lane = threadIdx.x & 63;
  const int wave = __builtin_amdgcn_readfirstlane(threadIdx.x >> 6);
  const int w = wave & 3;
  const u32 l16 = lane * 16;
  const u32 lds0 = (u32)(size_t)(LAS char*)smem;
  const rsrc_t wsr = __builtin_amdgcn_make_buffer_rsrc(ws, 0, WS_END, 0x00020000);
  const f32x4 z4 = {0, 0, 0, 0};
  auto tile = [&](int i) { return (w + 1 + i) & 3; };
  u32x2 xr[3];
  u32 xo;
  auto ldx = [&](int n0, int n1) {
    u32 l = l16;
    asm volatile("" : "+v"(l));
    xo = (rb * 64 + w * 16 + ((l >> 4) & 15)) * D_ + cb * 80 + (l >> 8) * 4;
#pragma unroll
    for (int n = n0; n < n1; n++) xr[n - n0] = *(const u32x2*)(x + xo + n * 16);
  };
  auto dpart = [&](int k, int n) { const int f = (3 - k) * 5 + n; return *(LAS f32x4*)(smem + p_off(f) + p_base((w + k) & 3, f) + l16); };
  auto spart = [&](int k, int n) { return *(LAS f32x4*)(smem + L_R + ((w + k) & 3) * R_SZ + ((3 - k) * 5 + n - (k == 0 ? 3 : 0)) * 1024 + l16); };
  auto finish = [&](f32x4 bs, f32x4 ss, u32x2 xv, int n) {
    float r[4];
#pragma unroll
    for (int j = 0; j < 4; j++) {
      const float v = bs[j], gt = v * __builtin_amdgcn_rcpf(1.f + __builtin_amdgcn_exp2f(-1.44269504089f * v));
      r[j] = ((j & 1) ? bhi(xv[j >> 1]) : blo(xv[j >> 1])) + ss[j] * gt;
    }
    *(u32x2*)(out + xo + n * 16) = u32x2{pk2(r[0], r[1]), pk2(r[2], r[3])};
  };
  if (wave < 4) {
    const int k0 = d_k0(w);
    const int ha = WS_HF + (rb * 54 + k0) * 4096, wa = WS_WDF + (cb * 54 + k0) * 5120;
    const int rot = d_rot();
    auto vof = [&](int s) { return (s == 3 || s == 8 || s == 13) ? l16 + ((w >= 2 && s + rot == 13) ? OOB : 0) : l16; };
    u32x4 a[14][4], b[14][5];
    auto lda = [&](int s) {
#pragma unroll
      for (int t = 0; t < 4; t++) a[s][t] = LD128(wsr, vof(s), ha + d_step(s, rot) * 4096 + tile(t) * 1024, LDAUX);
    };
    auto ldb = [&](int s) {
#pragma unroll
      for (int n = 0; n < 5; n++) b[s][n] = LD128(wsr, vof(s) + (n & 3) * 1024, wa + d_step(s, rot) * 5120 + (n >> 2) * 4096, LWAUX);
    };
    auto ldl = [&](int s) {
#pragma unroll
      for (int n = 0; n < 5; n++) { const int f = (s - 4) * 5 + n; b[s][n] = *(LAS u32x4*)(smem + p_off(f) + p_base(w, f) + l16); }
    };
    __builtin_amdgcn_s_setprio(3);
#pragma unroll
    for (int s = 0; s < 4; s++) ldb(s);
    SCHED();
    if (red >= 0) {
      const int hg = red >> 1, q = (red & 1) * 216 + threadIdx.x;
      group_wait<2>(smem, L_CNT + 32, w == 0, ws, WS_FKV + (hg * 16 + (lane & 7)) * 8, tag);
      STAMP(3);
      f32x4 kp[8];
#pragma unroll
      for (int p = 0; p < 8; p++)
        kp[p] = __builtin_bit_cast(f32x4, LD128(wsr, threadIdx.x < 216 && q < 420 ? q * 16 : OOB, WS_KVP + (hg * 8 + p) * 6720, SC1));
      const f32x4 t = ((kp[0] + kp[1]) + (kp[2] + kp[3])) + ((kp[4] + kp[5]) + (kp[6] + kp[7]));
      ST64((u32x2{pk2(t[0], t[1]), pk2(t[2], t[3])}), wsr, threadIdx.x < 216 ? q * 8 : OOB, WS_KVC + hg * 3456);
      arrive(smem, L_CNT + 8, 4, ws, WS_FK + red * 8, tag, 32, 128);
      lds_wait(smem, L_CNT + 8, 4);
      STAMP(5);
    }
    group_wait(smem, L_CNT + 36, w == 0, ws, WS_FG + (rb * 32 + (lane < 18 ? lane : 26 + (lane & 3))) * 8, tag);
    STAMP(8);
    SCHED();
#pragma unroll
    for (int s = 0; s < 5; s++) lda(s);
    SCHED();
    __builtin_amdgcn_s_setprio(DP_RUN);
    lds_wait(smem, L_CNT, 8);
    f32x4 acc[4][5];
#pragma unroll
    for (int s = 0; s < 14; s++) {
      SCHED();
#pragma unroll
      for (int t = 0; t < 4; t++)
#pragma unroll
        for (int n = 0; n < 5; n++) {
          acc[t][n] = MFMA16(b[s][n], a[s][t], (s == 0 ? z4 : acc[t][n]));
        }
      SCHED();
      if (s == 0) STAMP(9);
      if (s == 0) { lda(5); lda(6); }
      if (s == 1) { lda(7); }
      if (s == 2) { lda(8); }
      if (s == 3) { ldb(8); lda(9); }
      if (s == 4) { ldb(9); }
      if (s == 5) { lda(10); }
      if (s == 6) { ldb(10); }
      if (s == 7) { lda(11); ldb(11); }
      if (s == 8) { lda(12); ldb(12); }
      if (s == 9) { lda(13); ldb(13); }
      if (s == 1) ldl(4);
      if (s >= 3 && s <= 5) ldl(s + 2);
      if (s == 10) ldx(3, 5);
    }
    STAMP(10);
#pragma unroll
    for (int f = 0; f < 18; f++) *(LAS f32x4*)(smem + p_off(f) + p_base(w, f) + l16) = acc[f / 5][f % 5];
    WAIT_L0(); BAR();
    STAMP(11);
#pragma unroll
    for (int n = 3; n < 5; n++) {
      const f32x4 bs = (acc[3][n] + dpart(1, n)) + (dpart(2, n) + dpart(3, n));
      const f32x4 ss = (spart(0, n) + spart(1, n)) + (spart(2, n) + spart(3, n));
      finish(bs, ss, xr[n - 3], n);
    }
  } else {
    const int g = lane >> 4, c15 = lane & 15;
    const u32x4 srdp = mk_srd(ws, WS_END);
    const u32 rl = lds0 + L_R + w * R_SZ;
    __builtin_amdgcn_s_setprio(SP_W);
    u32x4 wf[5][5], pq[4][2][2];
    u32x4 ph[4];
    const int wa = WS_WOF + (cb * 20 + w * 5) * 5120;
#pragma unroll
    for (int s = 0; s < 3; s++)
#pragma unroll
      for (int n = 0; n < 5; n++) wf[s][n] = LD128(wsr, l16 + (n & 3) * 1024, wa + s * 5120 + (n >> 2) * 4096, LWAUX);
    asm volatile("s_nop 4" ::: "memory");
#pragma unroll
    for (int i = 0; i < 10; i++) DMA16C(rl + i * 1024, l16, srdp, wa + 3 * 5120 + i * 1024);
    SCHED();
    if (red >= 0) group_wait(smem, L_CNT + 44, w == 0, ws, WS_FG + (rb * 32 + 26 + (lane & 3)) * 8, tag);
    else lds_wait(smem, L_CNT + 36, 1);
    STAMP(7);
#pragma unroll
    for (int h = 0; h < 2; h++)
#pragma unroll
      for (int t = 0; t < 4; t++) {
        const int qa = WS_QF + ((rb * 4 + tile(t)) * 8 + 2 * w + h) * 2560;
        pq[t][h][0] = LD128(wsr, l16, qa, LDAUX); pq[t][h][1] = LD128(wsr, l16 + 1024, qa, LDAUX);
        const u32x2 v = LD64(wsr, lane * 8 + 2048, qa, LDAUX);
        ph[t][2 * h] = v[0]; ph[t][2 * h + 1] = v[1];
      }
    SCHED();
    constexpr int ga[18][2] = {{0, 0}, {0, 1}, {1, 0}, {1, 1}, {1, 2}, {2, 1}, {2, 2}, {2, 3}, {3, 2}, {3, 3}, {3, 4}, {4, 3}, {4, 4}, {5, 0}, {5, 1}, {5, 2}, {5, 3}, {5, 4}};
    u32 gad[18];
#pragma unroll
    for (int k = 0; k < 18; k++) {
      const int j = ga[k][0], e = 16 * j + c15, hd = j == 5 ? (c15 & 3) : e / 20, row = j == 5 ? 20 : e - hd * 20;
      const u32 d = 16 * ga[k][1] + 4 * g - 20 * hd;
      gad[k] = rl + 10240 + (d < 20u ? hd * 840 + row * 40 + d * 2 : 3360);
      asm volatile("" : "+v"(gad[k]));
    }
    __builtin_amdgcn_s_setprio(SP_POLL);
    group_wait(smem, L_CNT + 40, w == 0, ws, WS_FK + rb * 128 + (lane & 15) * 8, tag);
    __builtin_amdgcn_s_setprio(SP_HI);
    STAMP(8);
#pragma unroll
    for (int i = 0; i < 7; i++) DMA16C(rl + 10240 + i * 1024, l16, srdp, WS_KVC + w * 6912 + i * 1024);
    WAIT_V(0);
    STAMP(9);
    SCHED();
    {
      u32x2 gq[18];
      f32x4 on[2][6];
      const f32x4 e4 = {1e-6f, 1e-6f, 1e-6f, 1e-6f};
      auto fr2 = [&](int lo, int hi) { return u32x4{lo < 0 ? 0u : gq[lo][0], lo < 0 ? 0u : gq[lo][1], hi < 0 ? 0u : gq[hi][0], hi < 0 ? 0u : gq[hi][1]}; };
      auto mm = [&](int u) {
        const int h = u >> 2, t = u & 3;
        f32x4(&o)[6] = on[u & 1];
        const u32x4 p0 = pq[t][h][0], p1 = pq[t][h][1], p2 = ph[t];
        auto fr5 = [&](int k) { return h ? fr2(-1, k) : fr2(k, -1); };
        o[5] = MFMA16(fr2(13, 14), p0, e4);
        o[0] = MFMA16(fr2(0, 1), p0, z4);
        o[1] = MFMA16(fr2(2, 3), p0, z4);
        o[2] = MFMA16(fr2(-1, 5), p0, z4);
        o[5] = MFMA16(fr2(15, 16), p1, o[5]);
        o[3] = MFMA16(fr2(8, 9), p1, z4);
        o[4] = MFMA16(fr2(-1, 11), p1, z4);
        o[1] = MFMA16(fr2(4, -1), p1, o[1]);
        o[5] = MFMA16(fr5(17), p2, o[5]);
        o[2] = MFMA16(fr2(6, 7), p1, o[2]);
        o[3] = MFMA16(fr5(10), p2, o[3]);
        o[4] = MFMA16(fr5(12), p2, o[4]);
      };
      auto scale = [&](int u) {
        const int h = u >> 2, t = u & 3;
        f32x4(&o)[6] = on[u & 1];
        float rd[4];
#pragma unroll
        for (int j = 0; j < 4; j++) rd[j] = __builtin_amdgcn_rcpf(o[5][j]);
        const float rs[5] = {rd[0], g == 0 ? rd[0] : rd[1], g < 2 ? rd[1] : rd[2], g < 3 ? rd[2] : rd[3], rd[3]};
        u32 op[5][2];
#pragma unroll
        for (int j = 0; j < 5; j++) { op[j][0] = pk2(o[j][0] * rs[j], o[j][1] * rs[j]); op[j][1] = pk2(o[j][2] * rs[j], o[j][3] * rs[j]); }
        pq[t][h][0] = u32x4{op[0][0], op[0][1], op[1][0], op[1][1]};
        pq[t][h][1] = u32x4{op[2][0], op[2][1], op[3][0], op[3][1]};
        ph[t][2 * h] = op[4][0]; ph[t][2 * h + 1] = op[4][1];
      };
#pragma unroll
      for (int u = 0; u < 9; u++) {
        if ((u & 3) == 0 && u < 8) {
#pragma unroll
          for (int k = 0; k < 18; k++) gq[k] = *(LAS u32x2*)((LAS char*)(size_t)gad[k] + (u >> 2) * 3456);
          if (u > 0) { scale(u - 1); SCHED(); }
          mm(u);
        } else {
          if (u < 8) mm(u);
          scale(u - 1);
#pragma unroll
          for (int i = 0; i < 12; i++) { __builtin_amdgcn_sched_group_barrier(0x8, 1, 1); __builtin_amdgcn_sched_group_barrier(0x2, 3, 1); }
        }
        SCHED();
        if (u == 0) STAMP(13);
        if (u == 3) STAMP(14);
        if (u == 4) STAMP(5);
      }
    }
    STAMP(10);
#pragma unroll
    for (int i = 0; i < 10; i++) wf[3 + i / 5][i % 5] = *(LAS u32x4*)((LAS char*)(size_t)rl + i * 1024 + l16);
    ldx(0, 3);
    WAIT_L0();
    f32x4 acc[5];
#pragma unroll
    for (int t = 0; t < 4; t++) {
#pragma unroll
      for (int s = 0; s < 5; s++) {
        const u32x4 o = s < 4 ? pq[t][s >> 1][s & 1] : ph[t];
#pragma unroll
        for (int n = 0; n < 5; n++) acc[n] = MFMA16(wf[s][n], o, (s == 0 ? z4 : acc[n]));
      }
#pragma unroll
      for (int n = (t == 3 ? 3 : 0); n < 5; n++) *(LAS f32x4*)((LAS char*)(size_t)rl + (t * 5 + n - (t == 3 ? 3 : 0)) * 1024 + l16) = acc[n];
    }
    STAMP(12);
    WAIT_L0(); BAR();
    STAMP(11);
#pragma unroll
    for (int n = 0; n < 3; n++) {
      const f32x4 bs = (dpart(0, n) + dpart(1, n)) + (dpart(2, n) + dpart(3, n));
      const f32x4 ss = (acc[n] + spart(1, n)) + (spart(2, n) + spart(3, n));
      finish(bs, ss, xr[n], n);
    }
  }
}

__global__ __launch_bounds__(512, 2) void fused(const u16* __restrict__ x, const u16* __restrict__ wqkv, const u16* __restrict__ wo,
                                                const u16* __restrict__ wgt, const u16* __restrict__ wu, const u16* __restrict__ wd,
                                                u16* __restrict__ out, char* ws) {
  extern __shared__ __attribute__((aligned(16))) char smem[];
  STAMP_INIT(); STAMP(0);
  const int xcd = blockIdx.x & 7, idx = blockIdx.x >> 3;
  const int rb = (xcd >> 2) * 4 + (idx & 3), c = (idx >> 2) * 4 + (xcd & 3);
  const int rb2 = xcd * 4 + (idx >> 3), cb2 = idx & 7;
  u64 tag;
  if (threadIdx.x < 52) *(LAS u32x4*)(smem + L_CS + threadIdx.x * 16) = u32x4{0, 0, 0, 0};
  if (__builtin_amdgcn_readfirstlane(threadIdx.x >> 6) == 7) {
    u64 pc; asm volatile("s_getpc_b64 %0" : "=s"(pc));
    const u32x4 srdc = mk_srd((const void*)pc, CODEWARM);
    const u32 o = (idx & 7) * 8192 + (threadIdx.x & 63) * 128;
    asm volatile("s_mov_b32 m0, %0\n\ts_nop 4\n\tbuffer_load_dword %1, %2, 0 offen lds" ::"s"((u32)(size_t)(LAS char*)smem + L_SP + 16384), "v"(o), "s"(srdc) : "memory");
  }
  if (c >= 30) spare(smem, x, rb, c, cb2, ws, tag SP_PASS);
  else {
    const int lane = threadIdx.x & 63;
    const int wave = __builtin_amdgcn_readfirstlane(threadIdx.x >> 6);
    const int grp = wave >> 2, wg = wave & 3, img = wg >> 1;
    const u32 lds0 = (u32)(size_t)(LAS char*)smem;
    P1 p;
    const u32x4 srdx = mk_srd(x + (size_t)rb * 256 * D_, 256 * D_ * 2);
    u32 vx[2];
#pragma unroll
    for (int i = 0; i < 2; i++) { const int r = 8 * i + (lane >> 3); vx[i] = (64 * wg + r) * (D_ * 2) + (((lane & 7) ^ ((r >> 1) & 7)) << 4); }
    asm volatile("s_nop 4" ::: "memory");
#pragma unroll
    for (int i = 0; i < 8; i++) DMA16C(lds0 + grp * L_X1 + wg * 8192 + i * 1024, vx[i & 1], srdx, grp * 128 + (i >> 1) * 16 * D_ * 2);
    int col;
    if (c < 18) col = c * 96;
    else if (c < 26) col = 640 + img * 640 + 80 * (c - 18) - 16 * (c & 1);
    else col = 160 * (c - 26) + img * 64;
    p.pitch = c < 18 ? F_ * 2 : 3840;
    const u16* wb = (c < 18 ? (img ? wu : wgt) : wqkv) + col;
    const u32 wbytes = D_ * p.pitch - col * 2;
    const u32x4 srdw = mk_srd(wb, wbytes);
#pragma unroll
    for (int i = 0; i < 6; i++) { const int o = (6 * (wg & 1) + i) * 1024 + lane * 16, r = o / 192; p.vo[i] = r * p.pitch + ((o - r * 192) ^ ((r & 8) << 2)); }
    asm volatile("s_nop 4" ::: "memory");
#pragma unroll
    for (int i = 0; i < 6; i++) DMA16C(lds0 + (grp ? L_W1 : L_W0) + wg * 6144 + i * 1024, p.vo[i], srdw, grp * 64 * p.pitch);
    p.unit = (c * 8 + rb) * 4 + wg;
    if (grp == 0) {
      p.srd = srdx; p.vo[0] = vx[0]; p.vo[1] = vx[1];
      tag = get_tag(ws);
    } else {
#pragma unroll
      for (int i = 0; i < 6; i++) DMA16C(lds0 + L_W2 + wg * 6144 + i * 1024, p.vo[i], srdw, 2 * 64 * p.pitch);
      const int uu = p.unit < 740 ? p.unit : 739, uks = uu / 10, ucg = uu - uks * 10;
      int r0, r1;
      if (uks < 54) { r0 = 32 * uks; r1 = r0 + 16; }
      else {
        const int k = uks - 54, pr = k / 5, s5 = k - pr * 5;
        if (s5 < 4) { r0 = 160 * pr + 80 * (s5 >> 1) + 32 * (s5 & 1); r1 = r0 + 16; } else { r0 = 160 * pr + 64; r1 = r0 + 80; }
      }
      const u32x4 srdu = uks < 54 ? mk_srd(wd, F_ * D_ * 2) : mk_srd(wo, p.unit < 740 ? D_ * D_ * 2 : 0);
      const u32 vu = (lane >> 3) * (D_ * 2) + (lane & 7) * 16;
      asm volatile("s_nop 4" ::: "memory");
#pragma unroll
      for (int i = 0; i < 4; i++) DMA16(lds0 + L_SP + wg * 4096 + i * 1024, vu, srdu, ((i < 2 ? r0 : r1) + 8 * (i & 1)) * (D_ * 2) + ucg * 128);
      p.srd = srdw;
      tag = get_tag(ws);
    }
    STAMP(12);
    if (c < 18) phase1<0>(smem, p, 0, 0, rb, c, c, cb2, ws, tag SP_PASS);
    else if (c < 26) phase1<1>(smem, p, c & 1, c & 1, rb, c - 18, c, cb2, ws, tag SP_PASS);
    else phase1<2>(smem, p, 0, 1, rb, c - 26, c, cb2, ws, tag SP_PASS);
  }
  phase2(smem, x, out, rb2, cb2, c >= 30 ? rb * 2 + c - 30 : -1, ws, tag SP_PASS);
  STAMP(15); STAMP_FLUSH(ws);
}

extern "C" void kernel_launch(void* const* d_in, const int* in_sizes, int n_in, void* d_out, int out_size, void* d_ws, size_t ws_size,
                              hipStream_t stream) {
  (void)hipFuncSetAttribute((const void*)fused, hipFuncAttributeMaxDynamicSharedMemorySize, LDS_BYTES);
  fused<<<256, 512, LDS_BYTES, stream>>>((const u16*)d_in[0], (const u16*)d_in[1], (const u16*)d_in[2], (const u16*)d_in[4],
                                         (const u16*)d_in[5], (const u16*)d_in[6], (u16*)d_out, (char*)d_ws);
}
```

```cpp
#define TLBWARM 1048576
#include <hip/hip_runtime.h>
#include <type_traits>
#define LATE (c >= 18 && c < 26)
#define EPRIO 1
#define DP_RUN 0
#define SP_W 2
#define SP_HI 2
#define SP_POLL 2
#define CODEWARM 46000
typedef unsigned short u16;
typedef unsigned int u32;
typedef unsigned long long u64;
typedef short s16x4 __attribute__((ext_vector_type(4)));
typedef short s16x8 __attribute__((ext_vector_type(8)));
typedef __bf16 bf16x8 __attribute__((ext_vector_type(8)));
typedef __bf16 bf16x2 __attribute__((ext_vector_type(2)));
typedef float f32x2 __attribute__((ext_vector_type(2)));
typedef float f32x4 __attribute__((ext_vector_type(4)));
typedef u32 u32x2 __attribute__((ext_vector_type(2)));
typedef u32 u32x4 __attribute__((ext_vector_type(4)));
#define LAS __attribute__((address_space(3)))
#define GAS __attribute__((address_space(1)))
#define DI __device__ __forceinline__

constexpr int S_ = 2048, D_ = 640, F_ = 1728;
constexpr int WS_EPOCH = 0;
constexpr int WS_FG   = 4096;
constexpr int WS_FKV  = WS_FG + 8192;
constexpr int WS_FP   = WS_FKV + 1024;
constexpr int WS_FK   = 24576;
constexpr int WS_STAT = 16384;
constexpr int WS_KVC  = 32768;
constexpr int WS_KVP  = 65536;
constexpr int WS_HF   = 1 << 20;
constexpr int WS_QF   = WS_HF + 32 * 54 * 4096;
constexpr int WS_WDF  = WS_QF + 128 * 20 * 1024;
constexpr int WS_WOF  = WS_WDF + 8 * 54 * 5120;
constexpr int WS_END  = WS_WOF + 8 * 20 * 5120;
constexpr size_t WS_DBG = 64u << 20;
constexpr u64 TAG_STEP = 0x9E3779B97F4A7C15ull;
constexpr u32 SPIN_CAP = 1u << 16;
constexpr int SC1 = 16;
#define LDAUX 16
#define LWAUX 16
constexpr int OOB = 0x40000000;

constexpr int L_X0 = 0, L_W1 = 32768, L_W2 = 57344, L_X1 = 81920, L_W0 = 114688, L_SP = 139264;
constexpr int W_IMG1 = 12288;
constexpr int L_R = 81920, R_SZ = 19968;
constexpr int L_ST = 161792;
constexpr int L_CS = 162816;
constexpr int L_CNT = 163584;
constexpr int LDS_BYTES = 163840;
DI constexpr int p_off(int f) { return f < 8 ? L_X0 + f * 1024 : f < 14 ? L_W1 + (f - 8) * 1024 : L_W2 + (f - 14) * 1024; }
DI int p_base(int w, int f) { return w * (f < 8 ? 8192 : 6144); }

DI u32 pk2(float a, float b) { f32x2 v = {a, b}; return __builtin_bit_cast(u32, __builtin_convertvector(v, bf16x2)); }
DI float blo(u32 d) { return __uint_as_float(d << 16); }
DI float bhi(u32 d) { return __uint_as_float(d & 0xffff0000u); }
DI float rbf(float f) { return blo(pk2(f, f)); }
DI u32x4 mk_srd(const void* p, u32 bytes) {
  u64 a = (u64)p;
  u32x4 r;
  r[0] = __builtin_amdgcn_readfirstlane((u32)a); r[1] = __builtin_amdgcn_readfirstlane((u32)(a >> 32) & 0xffffu);
  r[2] = __builtin_amdgcn_readfirstlane(bytes); r[3] = 0x00020000u;
  return r;
}
#define DMA16(m0v, voff, srd, soff) \
  asm volatile("s_mov_b32 m0, %0\n\ts_nop 0\n\tbuffer_load_dwordx4 %1, %2, %3 offen lds" ::"s"(m0v), "v"(voff), "s"(srd), "s"(soff) : "memory")
#define DMA16C(m0v, voff, srd, soff) \
  asm volatile("s_mov_b32 m0, %0\n\ts_nop 0\n\tbuffer_load_dwordx4 %1, %2, %3 offen sc1 lds" ::"s"(m0v), "v"(voff), "s"(srd), "s"(soff) : "memory")
#define TOUCH(m0v, voff, srd, soff) \
  asm volatile("s_mov_b32 m0, %0\n\ts_nop 0\n\tbuffer_load_dword %1, %2, %3 offen lds" ::"s"(m0v), "v"(voff), "s"(srd), "s"(soff) : "memory")
#define WAIT_V(n) asm volatile("s_waitcnt vmcnt(" #n ")" ::: "memory")
#define WAIT_L0() do { asm volatile("" ::: "memory"); __builtin_amdgcn_s_waitcnt(0xC07F); asm volatile("" ::: "memory"); } while (0)
#define BAR() __builtin_amdgcn_s_barrier()
#define SCHED() __builtin_amdgcn_sched_barrier(0)
#define MFMA16(a, b, c) __builtin_amdgcn_mfma_f32_16x16x32_bf16(__builtin_bit_cast(bf16x8, a), __builtin_bit_cast(bf16x8, b), c, 0, 0, 0)
#define LD128(r, v, s, aux) __builtin_amdgcn_raw_buffer_load_b128(r, v, s, aux)
#define LD64(r, v, s, aux) __builtin_amdgcn_raw_buffer_load_b64(r, v, s, aux)
#define ST128(x, r, v, s) __builtin_amdgcn_raw_buffer_store_b128(x, r, (v) + (s), 0, SC1)
#define ST64(x, r, v, s) __builtin_amdgcn_raw_buffer_store_b64(x, r, (v) + (s), 0, SC1)
typedef __amdgpu_buffer_rsrc_t rsrc_t;

#define STAMP_INIT() do {} while (0)
#define STAMP(i) do {} while (0)
#define IVSTAMP(t) do {} while (0)
#define STAMP_FLUSH(ws) do {} while (0)
#define SP_ARG
#define SP_PASS

DI u64 flag_ld(char* ws, u32 off) { return __hip_atomic_load((GAS u64*)(ws + off), __ATOMIC_RELAXED, __HIP_MEMORY_SCOPE_AGENT); }
DI void flag_st(char* ws, u32 off, u64 v) { __hip_atomic_store((GAS u64*)(ws + off), v, __ATOMIC_RELAXED, __HIP_MEMORY_SCOPE_AGENT); }
template <int SLEEP = 2>
DI void flag_wait(char* ws, u32 off, u64 tag, u64 v) {
#pragma unroll 1
  for (u32 spins = 0; !__all(v == tag) && spins < SPIN_CAP; spins++) {
    __builtin_amdgcn_s_sleep(SLEEP);
    v = flag_ld(ws, off);
  }
  __builtin_amdgcn_fence(__ATOMIC_ACQUIRE, "wavefront");
}
DI void lds_wait(char* smem, int off, u32 want) {
#pragma unroll 1
  for (u32 spins = 0; spins < SPIN_CAP; spins++) {
    const u32 v = __hip_atomic_load((LAS u32*)(smem + off), __ATOMIC_RELAXED, __HIP_MEMORY_SCOPE_WORKGROUP);
    if (__builtin_amdgcn_readfirstlane(v) >= want) break;
    __builtin_amdgcn_s_sleep(1);
  }
  asm volatile("" ::: "memory");
}
#define PSLEEP 6
template <int SLEEP = PSLEEP>
DI void group_wait(char* smem, int word, bool leader, char* ws, u32 off, u64 tag) {
  if (leader) {
    flag_wait<SLEEP>(ws, off, tag, flag_ld(ws, off));
    *(volatile LAS u32*)(smem + word) = 1;
  } else lds_wait(smem, word, 1);
}
DI void arrive(char* smem, int cnt, u32 n, char* ws, u32 flag, u64 tag, int rep = 1, int stride = 0) {
  WAIT_V(0);
  const int lane = threadIdx.x & 63;
  u32 k = 0;
  if (lane == 0) k = __hip_atomic_fetch_add((LAS u32*)(smem + cnt), 1u, __ATOMIC_RELAXED, __HIP_MEMORY_SCOPE_WORKGROUP);
  if (__builtin_amdgcn_readfirstlane(k) == n - 1 && lane < rep) flag_st(ws, flag + lane * stride, tag);
}
DI u64 get_tag(char* ws) {
  u64 e;
  asm volatile("s_load_dwordx2 %0, %1, 0x0\n\ts_waitcnt lgkmcnt(0)" : "=s"(e) : "s"(ws + WS_EPOCH) : "memory");
  return e + TAG_STEP;
}
DI int d_k0(int w) { return w < 2 ? w * 14 : 2 + w * 13; }
DI int d_rot() {
  const int idx = blockIdx.x >> 3;
  return (((idx & 7) + (idx >> 3)) % 3) * 5;
}
DI int d_step(int s, int r) { return s + r >= 14 ? s + r - 14 : s + r; }
struct P1 {
  u32x4 srd;
  u32 vo[6];
  int pitch, unit;
};

template <int TYPE>
DI void phase1(char* smem, P1& p, int o0, int o1, int rb, int cb, int c, int cb2, char* ws, u64 tag SP_ARG) {
  constexpr int NT = TYPE == 0 ? 6 : 5;
  const int lane = threadIdx.x & 63;
  const int wave = __builtin_amdgcn_readfirstlane(threadIdx.x >> 6);
  const int grp = wave >> 2, wg = wave & 3, wm = wave >> 1, wn = wave & 1;
  const u32 lds0 = (u32)(size_t)(LAS char*)smem;
  const u32 mb = lds0 + wg * (grp == 0 ? 8192 : 6144);
  const int g = lane >> 4, c15 = lane & 15;
  const u32 l16 = lane * 16;
  constexpr int NB = TYPE == 1 ? 5 : TYPE == 0 ? 3 : 2;
  u32 ax[2], aw[NB];
  const int ksw = wm & 1;
  ax[0] = lds0 + (wm * 64 + c15) * 128 + (((4 * ksw + g) ^ (c15 >> 1)) << 4);
  ax[1] = ax[0] ^ 64;
  {
    const u32 awr = lds0 + (8 * g + (c15 >> 2)) * 192, sw = (g & 1) << 5, p8 = (lane & 3) * 8;
#pragma unroll
    for (int n = 0; n < NB; n++) {
      if (TYPE == 0) aw[n] = awr + (((n < 2 ? 4 * wn + n : 2 + wn) * 32 + p8) ^ sw);
      else if (TYPE == 2) aw[n] = awr + wn * W_IMG1 + ((((wn ? o1 : o0) + n) * 32 + p8) ^ sw);
      else {
        const int j = 32 * n + p8;
        aw[n] = awr + (j < 80 ? 0 : W_IMG1) + ((32 * o0 + 80 * wn + (j < 80 ? j : j - 80)) ^ sw);
      }
    }
  }
  float cs[NT];
#pragma unroll
  for (int n = 0; n < NT; n++) cs[n] = 0.f;
  f32x4 acc[4][NT];
#pragma unroll
  for (int m = 0; m < 4; m++)
#pragma unroll
    for (int n = 0; n < NT; n++) acc[m][n] = f32x4{0, 0, 0, 0};
  s16x8 xf[2][4], wf[2][NT];
  const rsrc_t wsr = __builtin_amdgcn_make_buffer_rsrc(ws, 0, WS_END, 0x00020000);
  const u32x4 srdp = mk_srd(ws, WS_END);
  const u32 psrc = WS_WDF + (cb2 * 54 + d_k0(wg) + d_step(4, d_rot())) * 5120;
  u32x4 srd = p.srd;
  u32 xstep = 16 * D_ * 2;

  auto trd = [&](LAS char* a) { return __builtin_amdgcn_ds_read_tr16_b64_v4i16((LAS s16x4*)a); };
  auto dma_x = [&](int i, u32 m, u32 so) { DMA16C(m + i * 1024, p.vo[i & 1], srd, so + (i >> 1) * xstep); };
  auto dma_w = [&](int i, u32 m, u32 so) { DMA16C(m + i * 1024, p.vo[i], srd, so); };
  auto load_phase = [&](auto mode, u32 xo, u32 wo, u32 m, u32 so) {
    constexpr int MODE = decltype(mode)::value;
#pragma unroll
    for (int k2 = 0; k2 < 2; k2++) {
      const u32 wk = wo + (k2 ^ ksw) * (32 * 192);
      u32 bw[NB], bx = ax[k2] + xo;
#pragma unroll
      for (int n = 0; n < NB; n++) { bw[n] = aw[n] + wk; asm volatile("" : "+v"(bw[n])); }
      asm volatile("" : "+v"(bx));
      if (MODE == 1) dma_x(4 * k2, m, so);
#pragma unroll
      for (int mm = 0; mm < 4; mm++) xf[k2][mm] = *(LAS s16x8*)((LAS char*)(size_t)bx + mm * 2048);
#pragma unroll
      for (int n = 0; n < NT; n++) {
        if (!(n & 1)) { if (MODE == 1) dma_x(4 * k2 + 1 + n / 2, m, so); if (MODE == 2) dma_w(3 * k2 + n / 2, m, so); }
        LAS char* a = (LAS char*)(size_t)bw[n % NB] + (TYPE == 0 ? (n / 3) * W_IMG1 : TYPE == 2 ? (n / 2) * 64 : 0);
        s16x4 lo = trd(a), hi = trd(a + 4 * 192);
        wf[k2][n] = __builtin_shufflevector(lo, hi, 0, 1, 2, 3, 4, 5, 6, 7);
      }
    }
  };
  auto compute = [&]() {
#pragma unroll
    for (int k2 = 0; k2 < 2; k2++)
#pragma unroll
      for (int m = 0; m < 4; m++)
#pragma unroll
        for (int n = 0; n < NT; n++)
          acc[m][n] = TYPE == 1 ? MFMA16(xf[k2][m], wf[k2][n], acc[m][n]) : MFMA16(wf[k2][n], xf[k2][m], acc[m][n]);
  };
#define CSMODE 2
#define CS_ALLA
  constexpr int CSA = NT;
  f32x4 cm[NT];
#pragma unroll
  for (int n = 0; n < NT; n++) cm[n] = f32x4{0, 0, 0, 0};
  auto colsum = [&](int n0, int n1) {
#pragma unroll
    for (int n = n0; n < n1; n++) {
      const u32x4 d = __builtin_bit_cast(u32x4, wf[0][n]);
      if (CSMODE == 0) {
        float s0 = blo(d[0]) + bhi(d[0]), s1 = blo(d[1]) + bhi(d[1]), s2 = blo(d[2]) + bhi(d[2]), s3 = blo(d[3]) + bhi(d[3]);
        cs[n] += (s0 + s1) + (s2 + s3);
      } else if (CSMODE == 1) {
        const bf16x2 one = {(__bf16)1.f, (__bf16)1.f};
#pragma unroll
        for (int j = 0; j < 4; j++) cs[n] = __builtin_amdgcn_fdot2_f32_bf16(__builtin_bit_cast(bf16x2, d[j]), one, cs[n], false);
      } else {
        const u32x4 ones = {0x3f803f80u, 0x3f803f80u, 0x3f803f80u, 0x3f803f80u};
        cm[n] = MFMA16(ones, d, cm[n]);
      }
    }
  };
  auto interleave = [&]() {
    if (CSMODE == 2) return;
#pragma unroll
    for (int i = 0; i < 8 * NT; i++) { __builtin_amdgcn_sched_group_barrier(0x8, CSMODE == 1 ? 3 : 1, 0); __builtin_amdgcn_sched_group_barrier(0x2, CSA == NT && CSMODE == 0 ? 2 : 1, 0); }
  };
  auto cs_out = [&](int n0, int n1) {
#pragma unroll
    for (int n = n0; n < n1; n++) {
      if (CSMODE == 2) {
        if (lane < 16) __hip_atomic_fetch_add((LAS float*)(smem + L_CS) + (wn * 6 + n) * 16 + lane, cm[n][0], __ATOMIC_RELAXED, __HIP_MEMORY_SCOPE_WORKGROUP);
        continue;
      }
      const auto a = __builtin_amdgcn_permlane32_swap(__float_as_uint(cs[n]), __float_as_uint(cs[n]), false, false);
      const float s = __uint_as_float(a[0]) + __uint_as_float(a[1]);
      const auto b = __builtin_amdgcn_permlane16_swap(__float_as_uint(s), __float_as_uint(s), false, false);
      const float r = __uint_as_float(b[0]) + __uint_as_float(b[1]);
      if (lane < 16) __hip_atomic_fetch_add((LAS float*)(smem + L_CS) + (wn * 6 + n) * 16 + lane, r, __ATOMIC_RELAXED, __HIP_MEMORY_SCOPE_WORKGROUP);
    }
  };
  auto wait_tile0 = [&]() {
    const u32 t32 = (u32)tag;
#pragma unroll 1
    for (u32 spins = 0; spins < (SPIN_CAP << 4); spins++) {
      const u32x4 v = *(volatile LAS u32x4*)(smem + L_CNT + 64);
      if (__all(v[0] == t32 && v[1] == t32 && v[2] == t32 && v[3] == t32)) break;
      __builtin_amdgcn_s_sleep(1);
    }
    asm volatile("" ::: "memory");
  };
  using I0 = std::integral_constant<int, 0>; using I1 = std::integral_constant<int, 1>; using I2 = std::integral_constant<int, 2>;
  asm volatile("" : "+v"(ax[0]), "+v"(ax[1]));
#pragma unroll
  for (int n = 0; n < NB; n++) asm volatile("" : "+v"(aw[n]));
  if (grp == 0) {
    u32 ua, ub, ud[4];
    {
      const int uu = p.unit < 740 ? p.unit : 739, uks = uu / 10, ucg = uu - uks * 10;
      const bool alt = uks < 54 && uks % 3 == 1;
      ua = lds0 + L_SP + wg * 4096 + ((alt ? 16 * (g >> 1) + 4 * (g & 1) : 4 * g) + (c15 >> 2)) * 128 + (lane & 3) * 8;
      ub = alt ? 8 * 128 : 16 * 128;
#pragma unroll
      for (int j = 0; j < 4; j++) {
        const int n16 = ucg * 4 + j, cbk = n16 / 5, nt = n16 - cbk * 5;
        ud[j] = p.unit >= 740 ? OOB : (uks < 54 ? WS_WDF + (cbk * 54 + uks) * 5120 : WS_WOF + (cbk * 20 + uks - 54) * 5120) + nt * 1024;
      }
      asm volatile("" : "+v"(ua), "+s"(ub), "+s"(ud[0]), "+s"(ud[1]), "+s"(ud[2]), "+s"(ud[3]));
    }
    WAIT_V(0);
    *(volatile LAS u32*)(smem + L_CNT + 64 + wg * 4) = (u32)tag;
    wait_tile0();
    STAMP(1);
    SCHED();
    load_phase(I0{}, L_X0, L_W0, 0, 0); SCHED(); compute(); colsum(0, CSA); interleave(); SCHED();
    BAR(); SCHED();
    STAMP(6);
    u32 w_cur = L_W1, w_nxt = L_W2, w_prv = L_W0, so = 2 * 128;
#pragma unroll 1
    for (int t = 1; t < 10; t++) {
      const u32 xo = (t & 1) * L_X1;
      if (t == 2) {
#pragma unroll
        for (int j = 0; j < 4; j++) {
          const s16x4 lo = trd((LAS char*)(size_t)ua + j * 32), hi = trd((LAS char*)(size_t)(ua + ub) + j * 32);
          ST128(__builtin_bit_cast(u32x4, __builtin_shufflevector(lo, hi, 0, 1, 2, 3, 4, 5, 6, 7)), wsr, l16, ud[j]);
        }
      }
      SCHED();
      load_phase(I1{}, xo, w_cur, mb + (L_X1 - xo), so);
      { const u32 u = w_prv; w_prv = w_cur; w_cur = w_nxt; w_nxt = u; }
      so += 128;
      SCHED(); compute(); colsum(0, CSA); interleave(); SCHED();
      if (t == 8) { srd = srdp; so = psrc; xstep = 2048; p.vo[0] = l16; p.vo[1] = l16 + 1024; }
      if (t == 3) STAMP(7);
      if (t == 7) STAMP(13);
      if (t == 8) STAMP(14);
      if (t < 9) WAIT_V(0); else { cs_out(0, CSA); WAIT_L0(); }
      if (t == 2) arrive(smem, L_CNT + 12, 4, ws, WS_FP + (c * 8 + rb) * 8, tag);
      BAR(); SCHED();
    }
  } else {
    __builtin_amdgcn_s_setprio(1);
#pragma unroll
    for (int m = 0; m < 4; m++)
#pragma unroll
      for (int n = 0; n < NT; n++) asm volatile("" : "+v"(acc[m][n]));
    wait_tile0();
    STAMP(1);
    SCHED();
    load_phase(I0{}, L_X0, L_W0, 0, 0); WAIT_L0(); WAIT_V(10); SCHED(); BAR(); SCHED();
    STAMP(6);
    u32 w_cur = L_W1, w_nxt = L_W2, w_prv = L_W0, so = 3 * 64 * p.pitch, sstep = 64 * p.pitch;
    const u32 fso = WS_FP + (wg * 64 + lane) * 8;
    u64 fv = 0;
#pragma unroll 1
    for (int t = 1; t < 10; t++) {
      compute(); colsum(CSA, NT); interleave(); SCHED();
      if (t == 7) asm volatile("global_load_dwordx2 %0, %1, %2 sc1" : "+v"(fv) : "v"(fso), "s"(ws) : "memory");
      if (t == 8) {
        srd = srdp; so = psrc + 8192; sstep = 6144;
#pragma unroll
        for (int i = 0; i < 6; i++) p.vo[i] = l16 + i * 1024;
        DMA16C(lds0 + L_ST, l16, srdp, WS_STAT + rb * 1024);
      }
      SCHED();
      load_phase(I2{}, (t & 1) * L_X1, w_cur, mb + w_prv, so);
      { const u32 u = w_prv; w_prv = w_cur; w_cur = w_nxt; w_nxt = u; }
      so += sstep;
      if (t == 9) { colsum(CSA, NT); cs_out(CSA, NT); }
      else asm volatile("s_waitcnt vmcnt(6)" : "+v"(fv) :: "memory");
      if (t == 7) { flag_wait(ws, fso, tag, fv); }
      WAIT_L0();
      SCHED(); BAR(); SCHED();
    }
    compute(); SCHED();
    __builtin_amdgcn_s_setprio(0);
  }
  STAMP(2);

  LAS float* csl = (LAS float*)(smem + L_CS) + wn * 96;
  LAS u32* stl = (LAS u32*)(smem + L_ST) + wm * 64;
  auto lo2 = [](f32x4 v) { return f32x2{v[0], v[1]}; };
  auto hi2 = [](f32x4 v) { return f32x2{v[2], v[3]}; };
  auto fma2 = [](f32x2 a, f32x2 b, f32x2 c) { return __builtin_elementwise_fma(a, b, c); };
  auto pkv = [](f32x2 v) { return __builtin_bit_cast(u32, __builtin_convertvector(v, bf16x2)); };
  auto phi2 = [&](f32x2 t, f32x2 rs, f32x2 rl, float c1 = 1.f + 1e-6f, float c2 = 1e-6f) {
    const f32x2 u = t * rl, lin = fma2(t, rs, f32x2{c1, c1});
    const f32x2 e = f32x2{__builtin_amdgcn_fmed3f(__builtin_amdgcn_exp2f(u[0]), 0.f, 1.f), __builtin_amdgcn_fmed3f(__builtin_amdgcn_exp2f(u[1]), 0.f, 1.f)} + f32x2{c2, c2};
    return f32x2{fmaxf(lin[0], e[0]), fmaxf(lin[1], e[1])};
  };
  if (TYPE != 1) {
    f32x4 cv[NT];
#pragma unroll
    for (int n = 0; n < NT; n++) cv[n] = *(LAS f32x4*)(csl + n * 16 + g * 4);
    u32 odd[4][2];
#pragma unroll
    for (int m = 0; m < 4; m++) {
      const u32 sv = stl[m * 16 + c15];
      const float mu1 = -blo(sv), r = __builtin_amdgcn_rcpf(bhi(sv));
      const f32x2 mu = {mu1, mu1};
      u32 o[5][2];
      if (TYPE == 0) {
        const f32x2 r2 = {r * r, r * r};
#pragma unroll
        for (int n = 0; n < 3; n++)
#pragma unroll
          for (int h = 0; h < 2; h++) {
            const f32x2 gt = fma2(mu, h ? hi2(cv[n]) : lo2(cv[n]), h ? hi2(acc[m][n]) : lo2(acc[m][n]));
            const f32x2 up = fma2(mu, h ? hi2(cv[(n + 3) % NT]) : lo2(cv[(n + 3) % NT]), h ? hi2(acc[m][(n + 3) % NT]) : lo2(acc[m][(n + 3) % NT]));
            o[n][h] = pkv(f32x2{fmaxf(gt[0], 0.f), fmaxf(gt[1], 0.f)} * (up * r2));
          }
        const int base = WS_HF + ((rb * 4 + wm) * 54 + cb * 3) * 4096;
        ST128((u32x4{o[0][0], o[0][1], o[1][0], o[1][1]}), wsr, l16 + m * 1024, base + wn * 8192);
        odd[m][0] = o[2][0]; odd[m][1] = o[2][1];
        if (m & 1) {
          const auto s0 = __builtin_amdgcn_permlane32_swap(odd[m - 1][0], odd[m][0], false, false);
          const auto s1 = __builtin_amdgcn_permlane32_swap(odd[m - 1][1], odd[m][1], false, false);
          ST128((u32x4{s0[0], s1[0], s0[1], s1[1]}), wsr, (lane >> 5) * 1024 + (lane & 31) * 16 + (m >> 1) * 2048, base + 4096 + wn * 512);
        }
      } else {
        const f32x2 rs = {r, r}, rl = {r * 1.44269504089f, r * 1.44269504089f};
#pragma unroll
        for (int n = 0; n < 5; n++) {
          o[n][0] = pkv(phi2(fma2(mu, lo2(cv[n]), lo2(acc[m][n])), rs, rl));
          o[n][1] = pkv(phi2(fma2(mu, hi2(cv[n]), hi2(acc[m][n])), rs, rl));
        }
        const int base = WS_QF + ((rb * 16 + wm * 4 + m) * 8 + cb * 2 + wn) * 2560;
        ST128((u32x4{o[0][0], o[0][1], o[1][0], o[1][1]}), wsr, l16, base);
        ST128((u32x4{o[2][0], o[2][1], o[3][0], o[3][1]}), wsr, l16 + 1024, base);
        ST64((u32x2{o[4][0], o[4][1]}), wsr, lane * 8 + 2048, base);
      }
    }
  } else {
    f32x2 cv[5];
#pragma unroll
    for (int n = 0; n < 5; n++) { const float v = csl[n * 16 + c15]; cv[n] = f32x2{v, v}; }
    const float mc1 = c15 < 8 ? 1.f + 1e-6f : 0.f, mc2 = c15 < 8 ? 1e-6f : -__builtin_inff();
    f32x4 kv[7], kz[3];
    const u32x4 ones = {0x3f803f80u, 0x3f803f80u, 0x3f803f80u, 0x3f803f80u};
    constexpr int ja[7] = {0, 0, 1, 1, 1, 2, 2}, jb[7] = {2, 3, 2, 3, 4, 3, 4};
#pragma unroll
    for (int mp = 0; mp < 2; mp++) {
      u32x4 fr[5];
#pragma unroll
      for (int mm = 0; mm < 2; mm++) {
        const int m = 2 * mp + mm;
        const u32x4 sv = *(LAS u32x4*)(stl + m * 16 + g * 4);
#pragma unroll
        for (int h = 0; h < 2; h++) {
          const f32x2 mu = {-blo(sv[2 * h]), -blo(sv[2 * h + 1])};
          const f32x2 rs = {__builtin_amdgcn_rcpf(bhi(sv[2 * h])), __builtin_amdgcn_rcpf(bhi(sv[2 * h + 1]))};
          const f32x2 rl = rs * f32x2{1.44269504089f, 1.44269504089f};
#pragma unroll
          for (int n = 0; n < 5; n++) {
            const f32x2 t = fma2(mu, cv[n], h ? hi2(acc[m][n]) : lo2(acc[m][n]));
            fr[n][2 * mm + h] = n < 2 ? pkv(phi2(t, rs, rl)) : n > 2 ? pkv(t * rs) : pkv(phi2(t, rs, rl, mc1, mc2));
          }
        }
      }
#pragma unroll
      for (int i = 0; i < 7; i++) kv[i] = MFMA16(fr[ja[i]], fr[jb[i]], (mp == 0 ? f32x4{0, 0, 0, 0} : kv[i]));
#pragma unroll
      for (int i = 0; i < 3; i++) kz[i] = MFMA16(fr[i], ones, (mp == 0 ? f32x4{0, 0, 0, 0} : kz[i]));
    }
    int ob[3], eb[3];
#pragma unroll
    for (int i = 0; i < 3; i++) {
      const int d = 16 * i + 4 * g, hl = d >= 20;
      ob[i] = L_R + wm * 6720 + (2 * wn + hl) * 1680 + (d - 20 * hl) * 4;
      eb[i] = d < 40 ? c15 - 40 - 20 * hl : 1000;
    }
#pragma unroll
    for (int i = 0; i < 7; i++) {
      const u32 e = eb[ja[i]] + 16 * jb[i];
      if (e < 20u) *(LAS f32x4*)(smem + ob[ja[i]] + e * 80) = kv[i];
    }
#pragma unroll
    for (int i = 0; i < 3; i++)
      if (c15 == 0 && eb[i] < 0) *(LAS f32x4*)(smem + ob[i] + 1600) = kz[i];
    WAIT_L0(); STAMP(5); BAR();
    if (threadIdx.x < 420) {
      LAS char* q = (LAS char*)smem + L_R + threadIdx.x * 16;
      const f32x4 t = (*(LAS f32x4*)q + *(LAS f32x4*)(q + 6720)) + (*(LAS f32x4*)(q + 2 * 6720) + *(LAS f32x4*)(q + 3 * 6720));
      ST128(__builtin_bit_cast(u32x4, t), wsr, threadIdx.x * 16, WS_KVP + (cb * 8 + rb) * 6720);
    }
  }
  STAMP(3);
  if (lane == 0) __hip_atomic_fetch_add((LAS u32*)(smem + L_CNT + 4), 1u, __ATOMIC_RELAXED, __HIP_MEMORY_SCOPE_WORKGROUP);
  arrive(smem, L_CNT, 8, ws, TYPE == 1 ? WS_FKV + (cb * 16 + rb) * 8 : WS_FG + (rb * 128 + c) * 8, tag, TYPE == 1 ? 1 : 4, 256);
  STAMP(4);
  lds_wait(smem, L_CNT, 8);
}

DI void spare(char* smem, const u16* x, int rb, int c, int cb2, char* ws, u64& tag SP_ARG) {
  const int lane = threadIdx.x & 63;
  const int wave = __builtin_amdgcn_readfirstlane(threadIdx.x >> 6);
  const int g = lane >> 4, c15 = lane & 15;
  const rsrc_t wsr = __builtin_amdgcn_make_buffer_rsrc(ws, 0, WS_END, 0x00020000);
  u32x4 xv[4][5];
  const int row0 = rb * 256 + (c - 30) * 128 + wave * 16;
#pragma unroll
  for (int p = 0; p < 4; p++)
#pragma unroll
    for (int i = 0; i < 5; i++) xv[p][i] = *(const u32x4*)(x + (row0 + 4 * p + g) * D_ + (c15 + 16 * i) * 8);
  SCHED();
  tag = get_tag(ws);
#pragma unroll 1
  for (int p = 0; p < 4; p++) {
    u32x4 v[5];
#pragma unroll
    for (int i = 0; i < 5; i++) { v[i] = xv[0][i]; xv[0][i] = xv[1][i]; xv[1][i] = xv[2][i]; xv[2][i] = xv[3][i]; }
    float s = 0;
#pragma unroll
    for (int i = 0; i < 5; i++)
#pragma unroll
      for (int j = 0; j < 4; j++) s += blo(v[i][j]) + bhi(v[i][j]);
#pragma unroll
    for (int o = 8; o; o >>= 1) s += __shfl_xor(s, o);
    const float m = s * (1.f / D_);
    float q = 0;
#pragma unroll
    for (int i = 0; i < 5; i++)
#pragma unroll
      for (int j = 0; j < 4; j++) { const float a = blo(v[i][j]) - m, b = bhi(v[i][j]) - m; q += a * a + b * b; }
#pragma unroll
    for (int o = 8; o; o >>= 1) q += __shfl_xor(q, o);
    const float sd = sqrtf(rbf(rbf(q * (1.f / D_)) + 1e-5f));
    __builtin_amdgcn_raw_buffer_store_b32(pk2(m, sd), wsr, c15 == 0 ? WS_STAT + (row0 + 4 * p + g) * 4 : OOB, 0, SC1);
  }
  WAIT_V(0); WAIT_L0(); BAR();
  if (threadIdx.x == 0) flag_st(ws, WS_FP + (c * 8 + rb) * 8, tag);
  STAMP(2);
  if (wave < 4) {
    const u32 fso = WS_FP + (wave * 64 + lane) * 8;
    flag_wait<16>(ws, fso, tag, flag_ld(ws, fso));
  }
  BAR();
  STAMP(13);
  if (c == 31 && rb == 0 && threadIdx.x == 0) flag_st(ws, WS_EPOCH, tag);
  if (wave < 4) {
    const u32x4 srdp = mk_srd(ws, WS_END);
    const u32 lds0 = (u32)(size_t)(LAS char*)smem, l16 = lane * 16;
    const u32 psrc = WS_WDF + (cb2 * 54 + d_k0(wave) + d_step(4, d_rot())) * 5120;
    asm volatile("s_nop 4" ::: "memory");
#pragma unroll
    for (int i = 0; i < 20; i++) DMA16C(lds0 + p_off(i) + p_base(wave, i), l16, srdp, psrc + i * 1024);
  }
  if (threadIdx.x == 0) *(volatile LAS u32*)(smem + L_CNT) = 8;
  STAMP(4);
}

DI void phase2(char* smem, const u16* x, u16* out, int rb, int cb, int red, char* ws, u64 tag SP_ARG) {
  const int lane = threadIdx.x & 63;
  const int wave = __builtin_amdgcn_readfirstlane(threadIdx.x >> 6);
  const int w = wave & 3;
  const u32 l16 = lane * 16;
  const u32 lds0 = (u32)(size_t)(LAS char*)smem;
  const rsrc_t wsr = __builtin_amdgcn_make_buffer_rsrc(ws, 0, WS_END, 0x00020000);
  const f32x4 z4 = {0, 0, 0, 0};
  auto tile = [&](int i) { return (w + 1 + i) & 3; };
  u32x2 xr[3];
  u32 xo;
  auto ldx = [&](int n0, int n1) {
    u32 l = l16;
    asm volatile("" : "+v"(l));
    xo = (rb * 64 + w * 16 + ((l >> 4) & 15)) * D_ + cb * 80 + (l >> 8) * 4;
#pragma unroll
    for (int n = n0; n < n1; n++) xr[n - n0] = *(const u32x2*)(x + xo + n * 16);
  };
  auto dpart = [&](int k, int n) { const int f = (3 - k) * 5 + n; return *(LAS f32x4*)(smem + p_off(f) + p_base((w + k) & 3, f) + l16); };
  auto spart = [&](int k, int n) { return *(LAS f32x4*)(smem + L_R + ((w + k) & 3) * R_SZ + ((3 - k) * 5 + n - (k == 0 ? 3 : 0)) * 1024 + l16); };
  auto finish = [&](f32x4 bs, f32x4 ss, u32x2 xv, int n) {
    float r[4];
#pragma unroll
    for (int j = 0; j < 4; j++) {
      const float v = bs[j], gt = v * __builtin_amdgcn_rcpf(1.f + __builtin_amdgcn_exp2f(-1.44269504089f * v));
      r[j] = ((j & 1) ? bhi(xv[j >> 1]) : blo(xv[j >> 1])) + ss[j] * gt;
    }
    *(u32x2*)(out + xo + n * 16) = u32x2{pk2(r[0], r[1]), pk2(r[2], r[3])};
  };
  if (wave < 4) {
    const int k0 = d_k0(w);
    const int ha = WS_HF + (rb * 54 + k0) * 4096, wa = WS_WDF + (cb * 54 + k0) * 5120;
    const int rot = d_rot();
    auto vof = [&](int s) { return (s == 3 || s == 8 || s == 13) ? l16 + ((w >= 2 && s + rot == 13) ? OOB : 0) : l16; };
    u32x4 a[14][4], b[14][5];
    auto lda = [&](int s) {
#pragma unroll
      for (int t = 0; t < 4; t++) a[s][t] = LD128(wsr, vof(s), ha + d_step(s, rot) * 4096 + tile(t) * 1024, LDAUX);
    };
    auto ldb = [&](int s) {
#pragma unroll
      for (int n = 0; n < 5; n++) b[s][n] = LD128(wsr, vof(s) + (n & 3) * 1024, wa + d_step(s, rot) * 5120 + (n >> 2) * 4096, LWAUX);
    };
    auto ldl = [&](int s) {
#pragma unroll
      for (int n = 0; n < 5; n++) { const int f = (s - 4) * 5 + n; b[s][n] = *(LAS u32x4*)(smem + p_off(f) + p_base(w, f) + l16); }
    };
    __builtin_amdgcn_s_setprio(3);
#pragma unroll
    for (int s = 0; s < 4; s++) ldb(s);
    SCHED();
    if (red >= 0) {
      const int hg = red >> 1, q = (red & 1) * 216 + threadIdx.x;
      group_wait<2>(smem, L_CNT + 32, w == 0, ws, WS_FKV + (hg * 16 + (lane & 7)) * 8, tag);
      STAMP(3);
      f32x4 kp[8];
#pragma unroll
      for (int p = 0; p < 8; p++)
        kp[p] = __builtin_bit_cast(f32x4, LD128(wsr, threadIdx.x < 216 && q < 420 ? q * 16 : OOB, WS_KVP + (hg * 8 + p) * 6720, SC1));
      const f32x4 t = ((kp[0] + kp[1]) + (kp[2] + kp[3])) + ((kp[4] + kp[5]) + (kp[6] + kp[7]));
      ST64((u32x2{pk2(t[0], t[1]), pk2(t[2], t[3])}), wsr, threadIdx.x < 216 ? q * 8 : OOB, WS_KVC + hg * 3456);
      arrive(smem, L_CNT + 8, 4, ws, WS_FK + red * 8, tag, 32, 128);
      lds_wait(smem, L_CNT + 8, 4);
      STAMP(5);
    }
    group_wait(smem, L_CNT + 36, w == 0, ws, WS_FG + (rb * 32 + (lane < 18 ? lane : 26 + (lane & 3))) * 8, tag);
    STAMP(8);
    SCHED();
#pragma unroll
    for (int s = 0; s < 5; s++) lda(s);
    SCHED();
    __builtin_amdgcn_s_setprio(DP_RUN);
    lds_wait(smem, L_CNT, 8);
    f32x4 acc[4][5];
#pragma unroll
    for (int s = 0; s < 14; s++) {
      SCHED();
#pragma unroll
      for (int t = 0; t < 4; t++)
#pragma unroll
        for (int n = 0; n < 5; n++) {
          acc[t][n] = MFMA16(b[s][n], a[s][t], (s == 0 ? z4 : acc[t][n]));
        }
      SCHED();
      if (s == 0) STAMP(9);
      if (s == 0) { lda(5); lda(6); }
      if (s == 1) { lda(7); }
      if (s == 2) { lda(8); }
      if (s == 3) { ldb(8); lda(9); }
      if (s == 4) { ldb(9); }
      if (s == 5) { lda(10); }
      if (s == 6) { ldb(10); }
      if (s == 7) { lda(11); ldb(11); }
      if (s == 8) { lda(12); ldb(12); }
      if (s == 9) { lda(13); ldb(13); }
      if (s == 1) ldl(4);
      if (s >= 3 && s <= 5) ldl(s + 2);
      if (s == 10) ldx(3, 5);
    }
    STAMP(10);
#pragma unroll
    for (int f = 0; f < 18; f++) *(LAS f32x4*)(smem + p_off(f) + p_base(w, f) + l16) = acc[f / 5][f % 5];
    WAIT_L0(); BAR();
    STAMP(11);
#pragma unroll
    for (int n = 3; n < 5; n++) {
      const f32x4 bs = (acc[3][n] + dpart(1, n)) + (dpart(2, n) + dpart(3, n));
      const f32x4 ss = (spart(0, n) + spart(1, n)) + (spart(2, n) + spart(3, n));
      finish(bs, ss, xr[n - 3], n);
    }
  } else {
    const int g = lane >> 4, c15 = lane & 15;
    const u32x4 srdp = mk_srd(ws, WS_END);
    const u32 rl = lds0 + L_R + w * R_SZ;
    __builtin_amdgcn_s_setprio(SP_W);
    u32x4 wf[5][5], pq[4][2][2];
    u32x4 ph[4];
    const int wa = WS_WOF + (cb * 20 + w * 5) * 5120;
#pragma unroll
    for (int s = 0; s < 3; s++)
#pragma unroll
      for (int n = 0; n < 5; n++) wf[s][n] = LD128(wsr, l16 + (n & 3) * 1024, wa + s * 5120 + (n >> 2) * 4096, LWAUX);
    asm volatile("s_nop 4" ::: "memory");
#pragma unroll
    for (int i = 0; i < 10; i++) DMA16C(rl + i * 1024, l16, srdp, wa + 3 * 5120 + i * 1024);
    SCHED();
    if (red >= 0) group_wait(smem, L_CNT + 44, w == 0, ws, WS_FG + (rb * 32 + 26 + (lane & 3)) * 8, tag);
    else lds_wait(smem, L_CNT + 36, 1);
    STAMP(7);
#pragma unroll
    for (int h = 0; h < 2; h++)
#pragma unroll
      for (int t = 0; t < 4; t++) {
        const int qa = WS_QF + ((rb * 4 + tile(t)) * 8 + 2 * w + h) * 2560;
        pq[t][h][0] = LD128(wsr, l16, qa, LDAUX); pq[t][h][1] = LD128(wsr, l16 + 1024, qa, LDAUX);
        const u32x2 v = LD64(wsr, lane * 8 + 2048, qa, LDAUX);
        ph[t][2 * h] = v[0]; ph[t][2 * h + 1] = v[1];
      }
    SCHED();
    constexpr int ga[18][2] = {{0, 0}, {0, 1}, {1, 0}, {1, 1}, {1, 2}, {2, 1}, {2, 2}, {2, 3}, {3, 2}, {3, 3}, {3, 4}, {4, 3}, {4, 4}, {5, 0}, {5, 1}, {5, 2}, {5, 3}, {5, 4}};
    u32 gad[18];
#pragma unroll
    for (int k = 0; k < 18; k++) {
      const int j = ga[k][0], e = 16 * j + c15, hd = j == 5 ? (c15 & 3) : e / 20, row = j == 5 ? 20 : e - hd * 20;
      const u32 d = 16 * ga[k][1] + 4 * g - 20 * hd;
      gad[k] = rl + 10240 + (d < 20u ? hd * 840 + row * 40 + d * 2 : 3360);
      asm volatile("" : "+v"(gad[k]));
    }
    __builtin_amdgcn_s_setprio(SP_POLL);
    group_wait(smem, L_CNT + 40, w == 0, ws, WS_FK + rb * 128 + (lane & 15) * 8, tag);
    __builtin_amdgcn_s_setprio(SP_HI);
    STAMP(8);
#pragma unroll
    for (int i = 0; i < 7; i++) DMA16C(rl + 10240 + i * 1024, l16, srdp, WS_KVC + w * 6912 + i * 1024);
    WAIT_V(0);
    STAMP(9);
    SCHED();
    {
      u32x2 gq[18];
      f32x4 on[2][6];
      const f32x4 e4 = {1e-6f, 1e-6f, 1e-6f, 1e-6f};
      auto fr2 = [&](int lo, int hi) { return u32x4{lo < 0 ? 0u : gq[lo][0], lo < 0 ? 0u : gq[lo][1], hi < 0 ? 0u : gq[hi][0], hi < 0 ? 0u : gq[hi][1]}; };
      auto mm = [&](int u) {
        const int h = u >> 2, t = u & 3;
        f32x4(&o)[6] = on[u & 1];
        const u32x4 p0 = pq[t][h][0], p1 = pq[t][h][1], p2 = ph[t];
        auto fr5 = [&](int k) { return h ? fr2(-1, k) : fr2(k, -1); };
        o[5] = MFMA16(fr2(13, 14), p0, e4);
        o[0] = MFMA16(fr2(0, 1), p0, z4);
        o[1] = MFMA16(fr2(2, 3), p0, z4);
        o[2] = MFMA16(fr2(-1, 5), p0, z4);
        o[5] = MFMA16(fr2(15, 16), p1, o[5]);
        o[3] = MFMA16(fr2(8, 9), p1, z4);
        o[4] = MFMA16(fr2(-1, 11), p1, z4);
        o[1] = MFMA16(fr2(4, -1), p1, o[1]);
        o[5] = MFMA16(fr5(17), p2, o[5]);
        o[2] = MFMA16(fr2(6, 7), p1, o[2]);
        o[3] = MFMA16(fr5(10), p2, o[3]);
        o[4] = MFMA16(fr5(12), p2, o[4]);
      };
      auto scale = [&](int u) {
        const int h = u >> 2, t = u & 3;
        f32x4(&o)[6] = on[u & 1];
        float rd[4];
#pragma unroll
        for (int j = 0; j < 4; j++) rd[j] = __builtin_amdgcn_rcpf(o[5][j]);
        const float rs[5] = {rd[0], g == 0 ? rd[0] : rd[1], g < 2 ? rd[1] : rd[2], g < 3 ? rd[2] : rd[3], rd[3]};
        u32 op[5][2];
#pragma unroll
        for (int j = 0; j < 5; j++) { op[j][0] = pk2(o[j][0] * rs[j], o[j][1] * rs[j]); op[j][1] = pk2(o[j][2] * rs[j], o[j][3] * rs[j]); }
        pq[t][h][0] = u32x4{op[0][0], op[0][1], op[1][0], op[1][1]};
        pq[t][h][1] = u32x4{op[2][0], op[2][1], op[3][0], op[3][1]};
        ph[t][2 * h] = op[4][0]; ph[t][2 * h + 1] = op[4][1];
      };
#pragma unroll
      for (int u = 0; u < 9; u++) {
        if ((u & 3) == 0 && u < 8) {
#pragma unroll
          for (int k = 0; k < 18; k++) gq[k] = *(LAS u32x2*)((LAS char*)(size_t)gad[k] + (u >> 2) * 3456);
          if (u > 0) { scale(u - 1); SCHED(); }
          mm(u);
        } else {
          if (u < 8) mm(u);
          scale(u - 1);
#pragma unroll
          for (int i = 0; i < 12; i++) { __builtin_amdgcn_sched_group_barrier(0x8, 1, 1); __builtin_amdgcn_sched_group_barrier(0x2, 3, 1); }
        }
        SCHED();
        if (u == 0) STAMP(13);
        if (u == 3) STAMP(14);
        if (u == 4) STAMP(5);
      }
    }
    STAMP(10);
#pragma unroll
    for (int i = 0; i < 10; i++) wf[3 + i / 5][i % 5] = *(LAS u32x4*)((LAS char*)(size_t)rl + i * 1024 + l16);
    ldx(0, 3);
    WAIT_L0();
    f32x4 acc[5];
#pragma unroll
    for (int t = 0; t < 4; t++) {
#pragma unroll
      for (int s = 0; s < 5; s++) {
        const u32x4 o = s < 4 ? pq[t][s >> 1][s & 1] : ph[t];
#pragma unroll
        for (int n = 0; n < 5; n++) acc[n] = MFMA16(wf[s][n], o, (s == 0 ? z4 : acc[n]));
      }
#pragma unroll
      for (int n = (t == 3 ? 3 : 0); n < 5; n++) *(LAS f32x4*)((LAS char*)(size_t)rl + (t * 5 + n - (t == 3 ? 3 : 0)) * 1024 + l16) = acc[n];
    }
    STAMP(12);
    WAIT_L0(); BAR();
    STAMP(11);
#pragma unroll
    for (int n = 0; n < 3; n++) {
      const f32x4 bs = (dpart(0, n) + dpart(1, n)) + (dpart(2, n) + dpart(3, n));
      const f32x4 ss = (acc[n] + spart(1, n)) + (spart(2, n) + spart(3, n));
      finish(bs, ss, xr[n], n);
    }
  }
}

__global__ __launch_bounds__(512, 2) void fused(const u16* __restrict__ x, const u16* __restrict__ wqkv, const u16* __restrict__ wo,
                                                const u16* __restrict__ wgt, const u16* __restrict__ wu, const u16* __restrict__ wd,
                                                u16* __restrict__ out, char* ws) {
  extern __shared__ __attribute__((aligned(16))) char smem[];
  STAMP_INIT(); STAMP(0);
  const int xcd = blockIdx.x & 7, idx = blockIdx.x >> 3;
  const int rb = (xcd >> 2) * 4 + (idx & 3), c = (idx >> 2) * 4 + (xcd & 3);
  const int rb2 = xcd * 4 + (idx >> 3), cb2 = idx & 7;
  u64 tag;
  if (threadIdx.x < 52) *(LAS u32x4*)(smem + L_CS + threadIdx.x * 16) = u32x4{0, 0, 0, 0};
  if (__builtin_amdgcn_readfirstlane(threadIdx.x >> 6) == 6) {
    const u32x4 srdw = mk_srd(ws, WS_END), srdo = mk_srd(out, S_ * D_ * 2);
    const u32 o = (threadIdx.x & 63) * TLBWARM + 524288;
    asm volatile("s_mov_b32 m0, %0\n\ts_nop 4\n\tbuffer_load_dword %1, %2, 0 offen lds" ::"s"((u32)(size_t)(LAS char*)smem + L_SP + 16384), "v"(o), "s"(srdw) : "memory");
    asm volatile("s_mov_b32 m0, %0\n\ts_nop 4\n\tbuffer_load_dword %1, %2, 0 offen lds" ::"s"((u32)(size_t)(LAS char*)smem + L_SP + 16384), "v"(o - 524288), "s"(srdo) : "memory");
  }
  if (__builtin_amdgcn_readfirstlane(threadIdx.x >> 6) == 7) {
    u64 pc; asm volatile("s_getpc_b64 %0" : "=s"(pc));
    const u32x4 srdc = mk_srd((const void*)pc, CODEWARM);
    const u32 o = (idx & 7) * 8192 + (threadIdx.x & 63) * 128;
    asm volatile("s_mov_b32 m0, %0\n\ts_nop 4\n\tbuffer_load_dword %1, %2, 0 offen lds" ::"s"((u32)(size_t)(LAS char*)smem + L_SP + 16384), "v"(o), "s"(srdc) : "memory");
  }
  if (c >= 30) spare(smem, x, rb, c, cb2, ws, tag SP_PASS);
  else {
    const int lane = threadIdx.x & 63;
    const int wave = __builtin_amdgcn_readfirstlane(threadIdx.x >> 6);
    const int grp = wave >> 2, wg = wave & 3, img = wg >> 1;
    const u32 lds0 = (u32)(size_t)(LAS char*)smem;
    P1 p;
    const u32x4 srdx = mk_srd(x + (size_t)rb * 256 * D_, 256 * D_ * 2);
    u32 vx[2];
#pragma unroll
    for (int i = 0; i < 2; i++) { const int r = 8 * i + (lane >> 3); vx[i] = (64 * wg + r) * (D_ * 2) + (((lane & 7) ^ ((r >> 1) & 7)) << 4); }
    asm volatile("s_nop 4" ::: "memory");
#pragma unroll
    for (int i = 0; i < 8; i++) DMA16C(lds0 + grp * L_X1 + wg * 8192 + i * 1024, vx[i & 1], srdx, grp * 128 + (i >> 1) * 16 * D_ * 2);
    int col;
    if (c < 18) col = c * 96;
    else if (c < 26) col = 640 + img * 640 + 80 * (c - 18) - 16 * (c & 1);
    else col = 160 * (c - 26) + img * 64;
    p.pitch = c < 18 ? F_ * 2 : 3840;
    const u16* wb = (c < 18 ? (img ? wu : wgt) : wqkv) + col;
    const u32 wbytes = D_ * p.pitch - col * 2;
    const u32x4 srdw = mk_srd(wb, wbytes);
#pragma unroll
    for (int i = 0; i < 6; i++) { const int o = (6 * (wg & 1) + i) * 1024 + lane * 16, r = o / 192; p.vo[i] = r * p.pitch + ((o - r * 192) ^ ((r & 8) << 2)); }
    asm volatile("s_nop 4" ::: "memory");
#pragma unroll
    for (int i = 0; i < 6; i++) DMA16C(lds0 + (grp ? L_W1 : L_W0) + wg * 6144 + i * 1024, p.vo[i], srdw, grp * 64 * p.pitch);
    p.unit = (c * 8 + rb) * 4 + wg;
    if (grp == 0) {
      p.srd = srdx; p.vo[0] = vx[0]; p.vo[1] = vx[1];
      tag = get_tag(ws);
    } else {
#pragma unroll
      for (int i = 0; i < 6; i++) DMA16C(lds0 + L_W2 + wg * 6144 + i * 1024, p.vo[i], srdw, 2 * 64 * p.pitch);
      const int uu = p.unit < 740 ? p.unit : 739, uks = uu / 10, ucg = uu - uks * 10;
      int r0, r1;
      if (uks < 54) { r0 = 32 * uks; r1 = r0 + 16; }
      else {
        const int k = uks - 54, pr = k / 5, s5 = k - pr * 5;
        if (s5 < 4) { r0 = 160 * pr + 80 * (s5 >> 1) + 32 * (s5 & 1); r1 = r0 + 16; } else { r0 = 160 * pr + 64; r1 = r0 + 80; }
      }
      const u32x4 srdu = uks < 54 ? mk_srd(wd, F_ * D_ * 2) : mk_srd(wo, p.unit < 740 ? D_ * D_ * 2 : 0);
      const u32 vu = (lane >> 3) * (D_ * 2) + (lane & 7) * 16;
      asm volatile("s_nop 4" ::: "memory");
#pragma unroll
      for (int i = 0; i < 4; i++) DMA16(lds0 + L_SP + wg * 4096 + i * 1024, vu, srdu, ((i < 2 ? r0 : r1) + 8 * (i & 1)) * (D_ * 2) + ucg * 128);
      p.srd = srdw;
      tag = get_tag(ws);
    }
    STAMP(12);
    if (c < 18) phase1<0>(smem, p, 0, 0, rb, c, c, cb2, ws, tag SP_PASS);
    else if (c < 26) phase1<1>(smem, p, c & 1, c & 1, rb, c - 18, c, cb2, ws, tag SP_PASS);
    else phase1<2>(smem, p, 0, 1, rb, c - 26, c, cb2, ws, tag SP_PASS);
  }
  phase2(smem, x, out, rb2, cb2, c >= 30 ? rb * 2 + c - 30 : -1, ws, tag SP_PASS);
  STAMP(15); STAMP_FLUSH(ws);
}

extern "C" void kernel_launch(void* const* d_in, const int* in_sizes, int n_in, void* d_out, int out_size, void* d_ws, size_t ws_size,
                              hipStream_t stream) {
  (void)hipFuncSetAttribute((const void*)fused, hipFuncAttributeMaxDynamicSharedMemorySize, LDS_BYTES);
  fused<<<256, 512, LDS_BYTES, stream>>>((const u16*)d_in[0], (const u16*)d_in[1], (const u16*)d_in[2], (const u16*)d_in[4],
                                         (const u16*)d_in[5], (const u16*)d_in[6], (u16*)d_out, (char*)d_ws);
}
```

```cpp
#include <hip/hip_runtime.h>
#include <type_traits>
#define LATE (c >= 18 && c < 26)
#define EPRIO 1
#define DP_RUN 0
#define SP_W 2
#define SP_HI 2
#define SP_POLL 2
#define CODEWARM 52000
typedef unsigned short u16;
typedef unsigned int u32;
typedef unsigned long long u64;
typedef short s16x4 __attribute__((ext_vector_type(4)));
typedef short s16x8 __attribute__((ext_vector_type(8)));
typedef __bf16 bf16x8 __attribute__((ext_vector_type(8)));
typedef __bf16 bf16x2 __attribute__((ext_vector_type(2)));
typedef float f32x2 __attribute__((ext_vector_type(2)));
typedef float f32x4 __attribute__((ext_vector_type(4)));
typedef u32 u32x2 __attribute__((ext_vector_type(2)));
typedef u32 u32x4 __attribute__((ext_vector_type(4)));
#define LAS __attribute__((address_space(3)))
#define GAS __attribute__((address_space(1)))
#define DI __device__ __forceinline__
#define LDS0 ((u32)__builtin_amdgcn_readfirstlane((u32)(size_t)smem))

constexpr int S_ = 2048, D_ = 640, F_ = 1728;
constexpr int WS_EPOCH = 0;
constexpr int WS_FG   = 4096;
constexpr int WS_FKV  = WS_FG + 8192;
constexpr int WS_FP   = WS_FKV + 1024;
constexpr int WS_FK   = 24576;
constexpr int WS_STAT = 491520 + 16384;
constexpr int WS_KVC  = 32768;
constexpr int WS_KVP  = 65536;
constexpr int WS_HF   = 1 << 20;
constexpr int WS_QF   = WS_HF + 32 * 54 * 4096;
constexpr int WS_WDF  = WS_QF + 128 * 20 * 1024;
constexpr int WS_WOF  = WS_WDF + 8 * 54 * 5120;
constexpr int WS_END  = WS_WOF + 8 * 20 * 5120;
constexpr size_t WS_DBG = 64u << 20;
constexpr u64 TAG_STEP = 0x9E3779B97F4A7C15ull;
constexpr u32 SPIN_CAP = 1u << 16;
constexpr int SC1 = 16;
#define LDAUX 16
#define LWAUX 16
constexpr int OOB = 0x40000000;

constexpr int L_X0 = 0, L_W1 = 32768, L_W2 = 57344, L_X1 = 81920, L_W0 = 114688, L_SP = 139264;
constexpr int W_IMG1 = 12288;
constexpr int L_R = 81920, R_SZ = 19968;
constexpr int L_ST = 155648;
constexpr int L_DUMMY = 161792;
constexpr int L_CS = 157696;
constexpr int L_CNT = 163584;
constexpr int LDS_BYTES = 163840;
DI constexpr int p_off(int f) { return f < 8 ? L_X0 + f * 1024 : f < 14 ? L_W1 + (f - 8) * 1024 : L_W2 + (f - 14) * 1024; }
DI int p_base(int w, int f) { return w * (f < 8 ? 8192 : 6144); }

DI u32 pk2(float a, float b) { f32x2 v = {a, b}; return __builtin_bit_cast(u32, __builtin_convertvector(v, bf16x2)); }
DI float blo(u32 d) { return __uint_as_float(d << 16); }
DI float bhi(u32 d) { return __uint_as_float(d & 0xffff0000u); }
DI float rbf(float f) { return blo(pk2(f, f)); }
DI u32x4 mk_srd(const void* p, u32 bytes) {
  u64 a = (u64)p;
  u32x4 r;
  r[0] = __builtin_amdgcn_readfirstlane((u32)a); r[1] = __builtin_amdgcn_readfirstlane((u32)(a >> 32) & 0xffffu);
  r[2] = __builtin_amdgcn_readfirstlane(bytes); r[3] = 0x00020000u;
  return r;
}
#define DMA16(m0v, voff, srd, soff) \
  asm volatile("s_mov_b32 m0, %0\n\ts_nop 0\n\tbuffer_load_dwordx4 %1, %2, %3 offen lds" ::"s"(m0v), "v"(voff), "s"(srd), "s"(soff) : "memory")
#define DMA16C(m0v, voff, srd, soff) \
  asm volatile("s_mov_b32 m0, %0\n\ts_nop 0\n\tbuffer_load_dwordx4 %1, %2, %3 offen sc1 lds" ::"s"(m0v), "v"(voff), "s"(srd), "s"(soff) : "memory")
#define TOUCH(m0v, voff, srd, soff) \
  asm volatile("s_mov_b32 m0, %0\n\ts_nop 0\n\tbuffer_load_dword %1, %2, %3 offen sc1 lds" ::"s"(m0v), "v"(voff), "s"(srd), "s"(soff) : "memory")
#define WAIT_V(n) asm volatile("s_waitcnt vmcnt(%0)" :: "i"(n) : "memory")
#define WAIT_L0() do { asm volatile("" ::: "memory"); __builtin_amdgcn_s_waitcnt(0xC07F); asm volatile("" ::: "memory"); } while (0)
#define BAR() __builtin_amdgcn_s_barrier()
#define SCHED() __builtin_amdgcn_sched_barrier(0)
#define MFMA16(a, b, c) __builtin_amdgcn_mfma_f32_16x16x32_bf16(__builtin_bit_cast(bf16x8, a), __builtin_bit_cast(bf16x8, b), c, 0, 0, 0)
#define LD128(r, v, s, aux) __builtin_amdgcn_raw_buffer_load_b128(r, v, s, aux)
#define LD64(r, v, s, aux) __builtin_amdgcn_raw_buffer_load_b64(r, v, s, aux)
#define ST128(x, r, v, s) __builtin_amdgcn_raw_buffer_store_b128(x, r, (v) + (s), 0, SC1)
#define ST64(x, r, v, s) __builtin_amdgcn_raw_buffer_store_b64(x, r, (v) + (s), 0, SC1)
typedef __amdgpu_buffer_rsrc_t rsrc_t;

#define STAMP_INIT() do {} while (0)
#define STAMP(i) do {} while (0)
#define IVSTAMP(t) do {} while (0)
#define STAMP_FLUSH(ws) do {} while (0)
#define TLS() do {} while (0)
#define SP_ARG
#define SP_PASS

DI u64 flag_ld(char* ws, u32 off) { return __hip_atomic_load((GAS u64*)(ws + off), __ATOMIC_RELAXED, __HIP_MEMORY_SCOPE_AGENT); }
DI void flag_st(char* ws, u32 off, u64 v) { __hip_atomic_store((GAS u64*)(ws + off), v, __ATOMIC_RELAXED, __HIP_MEMORY_SCOPE_AGENT); }
#define TMO_HIT(i, cond) do {} while (0)
template <int SLEEP = 2>
DI void flag_wait(char* ws, u32 off, u64 tag, u64 v) {
#pragma unroll 1
  for (u32 spins = 0; !__all(v == tag) && spins < SPIN_CAP; spins++) {
    __builtin_amdgcn_s_sleep(SLEEP);
    v = flag_ld(ws, off);
    __builtin_amdgcn_s_waitcnt(0x0F70);
    TMO_HIT(0, spins == SPIN_CAP - 1);
  }
  __builtin_amdgcn_fence(__ATOMIC_ACQUIRE, "wavefront");
}
DI void lds_wait(LAS char* smem, int off, u32 want) {
#pragma unroll 1
  for (u32 spins = 0; spins < SPIN_CAP; spins++) {
    const u32 v = __hip_atomic_load((LAS u32*)(smem + off), __ATOMIC_RELAXED, __HIP_MEMORY_SCOPE_WORKGROUP);
    if (__builtin_amdgcn_readfirstlane(v) >= want) break;
    __builtin_amdgcn_s_sleep(1);
    TMO_HIT(1, spins == SPIN_CAP - 1);
  }
  asm volatile("" ::: "memory");
}
#define SPSLEEP 48
#define NST 2
#define USKIP 0
#define ULEFT 2
#define UWAIT 6
#define DSLEEP 2
#define PSLEEP 6
template <int SLEEP = PSLEEP>
DI void group_wait(LAS char* smem, int word, bool leader, char* ws, u32 off, u64 tag) {
  if (leader) {
    flag_wait<SLEEP>(ws, off, tag, flag_ld(ws, off));
    *(volatile LAS u32*)(smem + word) = 1;
  } else lds_wait(smem, word, 1);
}
DI void arrive(LAS char* smem, int cnt, u32 n, char* ws, u32 flag, u64 tag, int rep = 1, int stride = 0) {
  WAIT_V(0);
  const int lane = threadIdx.x & 63;
  const u32 k = __hip_atomic_fetch_add((LAS u32*)(smem + (lane ? L_DUMMY + lane * 4 : cnt)), 1u, __ATOMIC_RELAXED, __HIP_MEMORY_SCOPE_WORKGROUP);
  const bool go = __builtin_amdgcn_readfirstlane(k) == n - 1 && lane < rep;
  const rsrc_t wsr = __builtin_amdgcn_make_buffer_rsrc(ws, 0, WS_END, 0x00020000);
  ST64((u32x2{(u32)tag, (u32)(tag >> 32)}), wsr, go ? flag + lane * stride : OOB, 0);
}
DI u64 get_tag(char* ws) {
  u64 e;
  asm volatile("s_load_dwordx2 %0, %1, 0x0\n\ts_waitcnt lgkmcnt(0)" : "=s"(e) : "s"(ws + WS_EPOCH) : "memory");
  return e + TAG_STEP;
}
DI int d_k0(int w) { return w < 2 ? w * 14 : 2 + w * 13; }
DI int d_rot() {
  const int idx = blockIdx.x >> 3;
  return (((idx & 7) + (idx >> 3)) % 3) * 5;
}
DI int d_step(int s, int r) { return s + r >= 14 ? s + r - 14 : s + r; }
struct P1 {
  u32x4 srd;
  u32 vo[6];
  u32 ua, ub, ud[4];
  int pitch;
};

template <int TYPE>
DI void phase1(LAS char* smem, P1& p, int o0, int o1, int rb, int cb, int c, int cb2, char* ws, u64 tag SP_ARG) {
  constexpr int NT = TYPE == 0 ? 6 : 5;
  const int lane = threadIdx.x & 63;
  const int wave = __builtin_amdgcn_readfirstlane(threadIdx.x >> 6);
  const int grp = wave >> 2, wg = wave & 3, wm = wave >> 1, wn = wave & 1;
  const u32 lds0 = LDS0;
  const u32 mb = lds0 + wg * (grp == 0 ? 8192 : 6144);
  const int g = lane >> 4, c15 = lane & 15;
  const u32 l16 = lane * 16;
  constexpr int NB = TYPE == 1 ? 5 : TYPE == 0 ? 3 : 2;
  u32 ax[2], aw[NB];
  const int ksw = wm & 1;
  ax[0] = lds0 + (wm * 64 + c15) * 128 + (((4 * ksw + g) ^ (c15 >> 1)) << 4);
  ax[1] = ax[0] ^ 64;
  {
    const u32 awr = lds0 + (8 * g + (c15 >> 2)) * 192, sw = (g & 1) << 5, p8 = (lane & 3) * 8;
#pragma unroll
    for (int n = 0; n < NB; n++) {
      if (TYPE == 0) aw[n] = awr + (((n < 2 ? 4 * wn + n : 2 + wn) * 32 + p8) ^ sw);
      else if (TYPE == 2) aw[n] = awr + wn * W_IMG1 + ((((wn ? o1 : o0) + n) * 32 + p8) ^ sw);
      else {
        const int j = 32 * n + p8;
        aw[n] = awr + (j < 80 ? 0 : W_IMG1) + ((32 * o0 + 80 * wn + (j < 80 ? j : j - 80)) ^ sw);
      }
    }
  }
  f32x4 acc[4][NT];
#pragma unroll
  for (int m = 0; m < 4; m++)
#pragma unroll
    for (int n = 0; n < NT; n++) acc[m][n] = f32x4{0, 0, 0, 0};
  s16x8 xf[2][4], wf[2][NT];
  const rsrc_t wsr = __builtin_amdgcn_make_buffer_rsrc(ws, 0, WS_END, 0x00020000);
  const u32x4 srdp = mk_srd(ws, WS_END);
  const u32 psrc = WS_WDF + (cb2 * 54 + d_k0(wg) + d_step(4, d_rot())) * 5120;
  u32 sr0 = __builtin_amdgcn_readfirstlane(p.srd[0]), sr1 = __builtin_amdgcn_readfirstlane(p.srd[1]), sr2 = __builtin_amdgcn_readfirstlane(p.srd[2]);
  u32 xstep = 16 * D_ * 2;

  auto trd = [&](LAS char* a) { return __builtin_amdgcn_ds_read_tr16_b64_v4i16((LAS s16x4*)a); };
  auto dma_x = [&](int i, u32 m, u32 so) { const u32x4 srd = {sr0, sr1, sr2, 0x00020000u}; DMA16C(m + i * 1024, p.vo[i & 1], srd, so + (i >> 1) * xstep); };
  auto dma_w = [&](int i, u32 m, u32 so) { const u32x4 srd = {sr0, sr1, sr2, 0x00020000u}; DMA16C(m + i * 1024, p.vo[i], srd, so); };
  u32 bw[NB], bx;
  auto load_q = [&](auto mode, int k2, int q, int d0, int dn, u32 xo, u32 wo, u32 m, u32 so) {
    constexpr int MODE = decltype(mode)::value;
    if (q == 0) {
      const u32 wk = wo + (k2 ^ ksw) * (32 * 192);
      bx = ax[k2] + xo;
#pragma unroll
      for (int n = 0; n < NB; n++) { bw[n] = aw[n] + wk; asm volatile("" : "+v"(bw[n])); }
      asm volatile("" : "+v"(bx));
    }
#pragma unroll
    for (int i = d0 + q * dn; i < d0 + (q + 1) * dn; i++) {
      if (MODE == 1 && i >= 0 && i < 8) dma_x(i, m, so);
      if (MODE == 2 && i >= 0 && i < 6) dma_w(i, m, so);
    }
    if (q == 0) {
#pragma unroll
      for (int mm = 0; mm < 4; mm++) xf[k2][mm] = *(LAS s16x8*)((LAS char*)(size_t)bx + mm * 2048);
    } else {
#pragma unroll
      for (int n = 2 * q - 2; n < 2 * q && n < NT; n++) {
        LAS char* a = (LAS char*)(size_t)bw[n % NB] + (TYPE == 0 ? (n / 3) * W_IMG1 : TYPE == 2 ? (n / 2) * 64 : 0);
        s16x4 lo = trd(a), hi = trd(a + 4 * 192);
        wf[k2][n] = __builtin_shufflevector(lo, hi, 0, 1, 2, 3, 4, 5, 6, 7);
      }
    }
  };
  auto load_half = [&](auto mode, int k2, int d0, int dn, u32 xo, u32 wo, u32 m, u32 so) {
#pragma unroll
    for (int q = 0; q < 4; q++) load_q(mode, k2, q, d0, dn, xo, wo, m, so);
  };
  auto load_phase = [&](auto mode, u32 xo, u32 wo, u32 m, u32 so) {
    constexpr int MODE = decltype(mode)::value;
    load_half(mode, 0, MODE == 1 ? 0 : -1, 1, xo, wo, m, so);
    load_half(mode, 1, MODE == 1 ? 4 : 3, 1, xo, wo, m, so);
  };
  auto comp_q = [&](int k2, int m) {
#pragma unroll
    for (int n = 0; n < NT; n++)
      acc[m][n] = TYPE == 1 ? MFMA16(xf[k2][m], wf[k2][n], acc[m][n]) : MFMA16(wf[k2][n], xf[k2][m], acc[m][n]);
  };
  auto comp_half = [&](int k2) {
#pragma unroll
    for (int m = 0; m < 4; m++) comp_q(k2, m);
  };
  auto compute = [&]() { comp_half(0); comp_half(1); };
  auto mixed = [&](int kc, u32 xo, u32 wo) {
#pragma unroll
    for (int q = 0; q < 4; q++) { load_q(std::integral_constant<int, 0>{}, 1 - kc, q, 0, 0, xo, wo, 0, 0); SCHED(); comp_q(kc, q); SCHED(); }
  };
  f32x4 cm[NT];
#pragma unroll
  for (int n = 0; n < NT; n++) cm[n] = f32x4{0, 0, 0, 0};
  const u32x4 ones = {0x3f803f80u, 0x3f803f80u, 0x3f803f80u, 0x3f803f80u};
  auto colsum = [&]() {
#pragma unroll
    for (int n = 0; n < NT; n++) cm[n] = MFMA16(ones, wf[0][n], cm[n]);
  };
  auto wait_tile0 = [&]() {
    const u32 t32 = (u32)tag;
#pragma unroll 1
    for (u32 spins = 0; spins < (SPIN_CAP << 4); spins++) {
      const u32x4 v = *(volatile LAS u32x4*)(smem + L_CNT + 64);
      if (__all(v[0] == t32 && v[1] == t32 && v[2] == t32 && v[3] == t32)) break;
      __builtin_amdgcn_s_sleep(1);
      TMO_HIT(2, spins == (SPIN_CAP << 4) - 1);
    }
    asm volatile("" ::: "memory");
  };
  using I0 = std::integral_constant<int, 0>; using I1 = std::integral_constant<int, 1>; using I2 = std::integral_constant<int, 2>;
  asm volatile("" : "+v"(ax[0]), "+v"(ax[1]));
#pragma unroll
  for (int n = 0; n < NB; n++) asm volatile("" : "+v"(aw[n]));
  int tl_ = 0; (void)tl_;
  if (grp == 0) {
    WAIT_V(0);
    *(volatile LAS u32*)(smem + L_CNT + 64 + wg * 4) = (u32)tag;
    wait_tile0();
    STAMP(1);
    SCHED();
    load_phase(I0{}, L_X0, L_W0, 0, 0); SCHED(); colsum(); compute(); SCHED();
    BAR(); SCHED();
    STAMP(6);
    u32 w_cur = L_W1, w_nxt = L_W2, w_prv = L_W0, so = 2 * 128, xo = L_X1;
    int left = 8;
#pragma unroll 1
    for (int seg = 0;; seg++) {
#pragma unroll 1
      for (;;) {
        TLS();
        load_phase(I1{}, xo, w_cur, mb + (L_X1 - xo), so);
        { const u32 u = w_prv; w_prv = w_cur; w_cur = w_nxt; w_nxt = u; }
        so += 128; xo = L_X1 - xo;
        TLS();
        SCHED(); colsum(); compute(); SCHED();
        TLS();
        if (--left == 0) break;
        WAIT_V(0); BAR(); SCHED();
      }
      if (seg) break;
      sr0 = srdp[0]; sr1 = srdp[1]; sr2 = srdp[2]; so = psrc; xstep = 2048; p.vo[0] = l16; p.vo[1] = l16 + 1024; left = 1;
      STAMP(14);
      WAIT_V(0); BAR(); SCHED();
    }
    BAR(); SCHED();
    if (lane < 16) {
#pragma unroll
      for (int n = 0; n < NT; n++) ((LAS float*)(smem + L_CS))[((ksw * 2 + wn) * 6 + n) * 16 + lane] = cm[n][0];
    }
    WAIT_L0();
    if (lane == 0) __hip_atomic_fetch_add((LAS u32*)(smem + L_CNT + 4), 1u, __ATOMIC_RELAXED, __HIP_MEMORY_SCOPE_WORKGROUP);
  } else {
    __builtin_amdgcn_s_setprio(1);
#pragma unroll
    for (int m = 0; m < 4; m++)
#pragma unroll
      for (int n = 0; n < NT; n++) asm volatile("" : "+v"(acc[m][n]));
    wait_tile0();
    STAMP(1);
    SCHED();
    load_phase(I2{}, L_X0, L_W0, mb + L_W2, 2 * 64 * p.pitch); WAIT_L0(); WAIT_V(UWAIT); SCHED(); BAR(); SCHED();
    STAMP(6);
    u32 w_cur = L_W1, w_nxt = L_W2, w_prv = L_W0, so = 3 * 64 * p.pitch, sstep = 64 * p.pitch, xo = L_X1;
    const u32 fso = WS_FP + l16;
    u32x4 fa = {0, 0, 0, 0}, fb = fa;
    int left = ULEFT;
    u32 uq[4 + USKIP];
#pragma unroll
    for (int j = 0; j < 4 + USKIP; j++) uq[j] = j < USKIP ? OOB : p.ud[j - USKIP];
    p.ua -= USKIP * 32; p.ub -= USKIP * 32;
    compute(); SCHED();
#pragma unroll 1
    for (int seg = 0;; seg++) {
#pragma unroll 1
      do {
        TLS();
        load_phase(I2{}, xo, w_cur, mb + w_prv, so);
        { const u32 u = w_prv; w_prv = w_cur; w_cur = w_nxt; w_nxt = u; }
        so += sstep; xo = L_X1 - xo;
        {
#pragma unroll
          for (int j = 0; j < NST; j++) {
            const s16x4 lo = trd((LAS char*)(size_t)p.ua + j * 32), hi = trd((LAS char*)(size_t)p.ub + j * 32);
            ST128(__builtin_bit_cast(u32x4, __builtin_shufflevector(lo, hi, 0, 1, 2, 3, 4, 5, 6, 7)), wsr, l16, uq[j]);
          }
          p.ua += NST * 32; p.ub += NST * 32;
#pragma unroll
          for (int j = 0; j < 4 + USKIP; j++) uq[j] = j + NST < 4 + USKIP ? uq[j + NST] : OOB;
        }
        WAIT_L0();
        TLS();
        asm volatile("s_waitcnt vmcnt(%2)" : "+v"(fa), "+v"(fb) : "i"(6 + NST) : "memory");
        TLS();
        SCHED(); BAR(); SCHED();
        compute(); SCHED();
      } while (--left);
      if (seg == 3) break;
      left = seg == 0 ? 6 - ULEFT : seg;
      if (seg == 0) arrive(smem, L_CNT + 12, 4, ws, WS_FP + (c * 8 + rb) * 8, tag);
      SCHED();
      if (seg == 1)
        asm volatile("global_load_dwordx4 %0, %2, %3 sc1\n\tglobal_load_dwordx4 %1, %2, %3 offset:1024 sc1" : "+v"(fa), "+v"(fb) : "v"(fso), "s"(ws) : "memory");
      if (seg == 2) {
        const u32 t0 = (u32)tag, t1 = (u32)(tag >> 32);
        const u32 bad = ((fa[0] ^ t0) | (fa[1] ^ t1) | (fa[2] ^ t0) | (fa[3] ^ t1)) | ((fb[0] ^ t0) | (fb[1] ^ t1) | (fb[2] ^ t0) | (fb[3] ^ t1));
        if (__builtin_amdgcn_ballot_w64(bad != 0) != 0) {
#pragma unroll 1
          for (int i = 0; i < 4; i++) flag_wait<4>(ws, WS_FP + i * 512 + lane * 8, tag, flag_ld(ws, WS_FP + i * 512 + lane * 8));
          __builtin_amdgcn_s_waitcnt(0x0F70);
        }
        sr0 = srdp[0]; sr1 = srdp[1]; sr2 = srdp[2]; so = psrc + 8192; sstep = 6144;
        DMA16C(lds0 + L_ST + (wg & 1) * 1024, l16, srdp, WS_STAT + rb * 2048 + (wg & 1) * 1024);
#pragma unroll
        for (int i = 0; i < 6; i++) p.vo[i] = l16 + i * 1024;
      }
      SCHED();
    }
    __builtin_amdgcn_s_setprio(0);
  }
  STAMP(2);

  lds_wait(smem, L_CNT + 4, 4);
  LAS float* csl = (LAS float*)(smem + L_CS) + wn * 96;
  LAS float* stl = (LAS float*)(smem + L_ST) + wm * 64;
  auto lo2 = [](f32x4 v) { return f32x2{v[0], v[1]}; };
  auto hi2 = [](f32x4 v) { return f32x2{v[2], v[3]}; };
  auto fma2 = [](f32x2 a, f32x2 b, f32x2 c) { return __builtin_elementwise_fma(a, b, c); };
  auto pkv = [](f32x2 v) { return __builtin_bit_cast(u32, __builtin_convertvector(v, bf16x2)); };
  auto phi2 = [&](f32x2 t, f32x2 rs, f32x2 rl, float c1 = 1.f + 1e-6f, float c2 = 1e-6f) {
    const f32x2 u = t * rl, lin = fma2(t, rs, f32x2{c1, c1});
    const f32x2 e = f32x2{__builtin_amdgcn_fmed3f(__builtin_amdgcn_exp2f(u[0]), 0.f, 1.f), __builtin_amdgcn_fmed3f(__builtin_amdgcn_exp2f(u[1]), 0.f, 1.f)} + f32x2{c2, c2};
    return f32x2{fmaxf(lin[0], e[0]), fmaxf(lin[1], e[1])};
  };
  if (TYPE != 1) {
    f32x4 cv[NT];
#pragma unroll
    for (int n = 0; n < NT; n++) cv[n] = *(LAS f32x4*)(csl + n * 16 + g * 4) + *(LAS f32x4*)(csl + 192 + n * 16 + g * 4);
    u32 odd[4][2];
#pragma unroll
    for (int m = 0; m < 4; m++) {
      const float mu1 = stl[m * 16 + c15], r = stl[256 + m * 16 + c15];
      const f32x2 mu = {mu1, mu1};
      u32 o[5][2];
      if (TYPE == 0) {
        const f32x2 r2 = {r * r, r * r};
#pragma unroll
        for (int n = 0; n < 3; n++)
#pragma unroll
          for (int h = 0; h < 2; h++) {
            const f32x2 gt = fma2(mu, h ? hi2(cv[n]) : lo2(cv[n]), h ? hi2(acc[m][n]) : lo2(acc[m][n]));
            const f32x2 up = fma2(mu, h ? hi2(cv[(n + 3) % NT]) : lo2(cv[(n + 3) % NT]), h ? hi2(acc[m][(n + 3) % NT]) : lo2(acc[m][(n + 3) % NT]));
            o[n][h] = pkv(f32x2{fmaxf(gt[0], 0.f), fmaxf(gt[1], 0.f)} * (up * r2));
          }
        const int base = WS_HF + ((rb * 4 + wm) * 54 + cb * 3) * 4096;
        ST128((u32x4{o[0][0], o[0][1], o[1][0], o[1][1]}), wsr, l16 + m * 1024, base + wn * 8192);
        odd[m][0] = o[2][0]; odd[m][1] = o[2][1];
        if (m & 1) {
          const auto s0 = __builtin_amdgcn_permlane32_swap(odd[m - 1][0], odd[m][0], false, false);
          const auto s1 = __builtin_amdgcn_permlane32_swap(odd[m - 1][1], odd[m][1], false, false);
          ST128((u32x4{s0[0], s1[0], s0[1], s1[1]}), wsr, (lane >> 5) * 1024 + (lane & 31) * 16 + (m >> 1) * 2048, base + 4096 + wn * 512);
        }
      } else {
        const f32x2 rs = {r, r}, rl = {r * 1.44269504089f, r * 1.44269504089f};
#pragma unroll
        for (int n = 0; n < 5; n++) {
          o[n][0] = pkv(phi2(fma2(mu, lo2(cv[n]), lo2(acc[m][n])), rs, rl));
          o[n][1] = pkv(phi2(fma2(mu, hi2(cv[n]), hi2(acc[m][n])), rs, rl));
        }
        const int base = WS_QF + ((rb * 16 + wm * 4 + m) * 8 + cb * 2 + wn) * 2560;
        ST128((u32x4{o[0][0], o[0][1], o[1][0], o[1][1]}), wsr, l16, base);
        ST128((u32x4{o[2][0], o[2][1], o[3][0], o[3][1]}), wsr, l16 + 1024, base);
        ST64((u32x2{o[4][0], o[4][1]}), wsr, lane * 8 + 2048, base);
      }
    }
  } else {
    f32x2 cv[5];
#pragma unroll
    for (int n = 0; n < 5; n++) { const float v = csl[n * 16 + c15] + csl[192 + n * 16 + c15]; cv[n] = f32x2{v, v}; }
    const float mc1 = c15 < 8 ? 1.f + 1e-6f : 0.f, mc2 = c15 < 8 ? 1e-6f : -__builtin_inff();
    f32x4 kv[7], kz[3];
    constexpr int ja[7] = {0, 0, 1, 1, 1, 2, 2}, jb[7] = {2, 3, 2, 3, 4, 3, 4};
#pragma unroll
    for (int mp = 0; mp < 2; mp++) {
      u32x4 fr[5];
#pragma unroll
      for (int mm = 0; mm < 2; mm++) {
        const int m = 2 * mp + mm;
        const f32x4 mu4 = *(LAS f32x4*)(stl + m * 16 + g * 4), rs4 = *(LAS f32x4*)(stl + 256 + m * 16 + g * 4);
#pragma unroll
        for (int h = 0; h < 2; h++) {
          const f32x2 mu = h ? hi2(mu4) : lo2(mu4), rs = h ? hi2(rs4) : lo2(rs4);
          const f32x2 rl = rs * f32x2{1.44269504089f, 1.44269504089f};
#pragma unroll
          for (int n = 0; n < 5; n++) {
            const f32x2 t = fma2(mu, cv[n], h ? hi2(acc[m][n]) : lo2(acc[m][n]));
            fr[n][2 * mm + h] = n < 2 ? pkv(phi2(t, rs, rl)) : n > 2 ? pkv(t * rs) : pkv(phi2(t, rs, rl, mc1, mc2));
          }
        }
      }
#pragma unroll
      for (int i = 0; i < 7; i++) kv[i] = MFMA16(fr[ja[i]], fr[jb[i]], (mp == 0 ? f32x4{0, 0, 0, 0} : kv[i]));
#pragma unroll
      for (int i = 0; i < 3; i++) kz[i] = MFMA16(fr[i], ones, (mp == 0 ? f32x4{0, 0, 0, 0} : kz[i]));
    }
    int ob[3], eb[3];
#pragma unroll
    for (int i = 0; i < 3; i++) {
      const int d = 16 * i + 4 * g, hl = d >= 20;
      ob[i] = L_R + wm * 6720 + (2 * wn + hl) * 1680 + (d - 20 * hl) * 4;
      eb[i] = d < 40 ? c15 - 40 - 20 * hl : 1000;
    }
#pragma unroll
    for (int i = 0; i < 7; i++) {
      const u32 e = eb[ja[i]] + 16 * jb[i];
      if (e < 20u) *(LAS f32x4*)(smem + ob[ja[i]] + e * 80) = kv[i];
    }
#pragma unroll
    for (int i = 0; i < 3; i++)
      if (c15 == 0 && eb[i] < 0) *(LAS f32x4*)(smem + ob[i] + 1600) = kz[i];
    WAIT_L0(); STAMP(5); BAR();
    if (threadIdx.x < 420) {
      LAS char* q = smem + L_R + threadIdx.x * 16;
      const f32x4 t = (*(LAS f32x4*)q + *(LAS f32x4*)(q + 6720)) + (*(LAS f32x4*)(q + 2 * 6720) + *(LAS f32x4*)(q + 3 * 6720));
      ST128(__builtin_bit_cast(u32x4, t), wsr, threadIdx.x * 16, WS_KVP + (cb * 8 + rb) * 6720);
    }
  }
  STAMP(3);
  arrive(smem, L_CNT, 8, ws, TYPE == 1 ? WS_FKV + (cb * 16 + rb) * 8 : WS_FG + (rb * 128 + c) * 8, tag, TYPE == 1 ? 1 : 4, 256);
  STAMP(4);
  lds_wait(smem, L_CNT, 8);
}

DI void spare(LAS char* smem, const u16* x, const u16* wqkv, const u16* wgt, const u16* wu, int rb, int c, int cb2, char* ws, u64& tag SP_ARG) {
  const int lane = threadIdx.x & 63;
  const int wave = __builtin_amdgcn_readfirstlane(threadIdx.x >> 6);
  const int g = lane >> 4, c15 = lane & 15;
  const rsrc_t wsr = __builtin_amdgcn_make_buffer_rsrc(ws, 0, WS_END, 0x00020000);
#define WROW0 352
#define WPRE0 192
#define WPREN 0
  const int gw = ((c - 30) * 8 + rb) * 8 + wave, mat = gw < 45 ? 0 : gw < 86 ? 1 : 2;
  const int pitch = mat ? F_ * 2 : 3840;
  const u32x4 sr = mk_srd(mat == 0 ? wqkv : mat == 1 ? wgt : wu, D_ * pitch);
  auto wtouch = [&](int r0, int n) {
    const u32 v = (gw - (mat == 0 ? 0 : mat == 1 ? 45 : 86)) * 8192 + lane * 128 + r0 * pitch;
    asm volatile("s_nop 4" ::: "memory");
#pragma unroll
    for (int j = 0; j < n; j++) TOUCH(LDS0 + L_DUMMY, v + j * 96 * pitch, sr, 0);
  };
  wtouch(WPRE0, WPREN);
  u32x4 xv[4][5];
  const int row0 = rb * 256 + (c - 30) * 128 + wave * 16;
#pragma unroll
  for (int p = 0; p < 4; p++)
#pragma unroll
    for (int i = 0; i < 5; i++) xv[p][i] = *(const u32x4*)(x + (row0 + 4 * p + g) * D_ + (c15 + 16 * i) * 8);
  SCHED();
  tag = get_tag(ws);
  auto row_sum = [](float v) {
    v += __builtin_bit_cast(float, __builtin_amdgcn_mov_dpp(__builtin_bit_cast(int, v), 0xB1, 0xF, 0xF, true));
    v += __builtin_bit_cast(float, __builtin_amdgcn_mov_dpp(__builtin_bit_cast(int, v), 0x4E, 0xF, 0xF, true));
    v += __builtin_bit_cast(float, __builtin_amdgcn_mov_dpp(__builtin_bit_cast(int, v), 0x141, 0xF, 0xF, true));
    v += __builtin_bit_cast(float, __builtin_amdgcn_mov_dpp(__builtin_bit_cast(int, v), 0x140, 0xF, 0xF, true));
    return v;
  };
#pragma unroll
  for (int p = 0; p < 4; p++) {
    float f[40], s0 = 0, s1 = 0;
#pragma unroll
    for (int i = 0; i < 20; i++) { f[2 * i] = blo(xv[p][i >> 2][i & 3]); f[2 * i + 1] = bhi(xv[p][i >> 2][i & 3]); s0 += f[2 * i]; s1 += f[2 * i + 1]; }
    const float m = row_sum(s0 + s1) * (1.f / D_);
    float q0 = 0, q1 = 0;
#pragma unroll
    for (int i = 0; i < 20; i++) { const float a = f[2 * i] - m, b = f[2 * i + 1] - m; q0 = __builtin_fmaf(a, a, q0); q1 = __builtin_fmaf(b, b, q1); }
    const float q = row_sum(q0 + q1);
    const float sd = rbf(sqrtf(rbf(rbf(q * (1.f / D_)) + 1e-5f)));
    __builtin_amdgcn_raw_buffer_store_b32(__float_as_uint(c15 ? 1.f / sd : -rbf(m)), wsr,
                                          c15 < 2 ? WS_STAT + rb * 2048 + c15 * 1024 + ((row0 & 255) + 4 * p + g) * 4 : OOB, 0, SC1);
    SCHED();
  }
  WAIT_V(0); WAIT_L0(); BAR();
  if (threadIdx.x == 0) flag_st(ws, WS_FP + (c * 8 + rb) * 8, tag);
  STAMP(2);
  wtouch(WROW0, (640 - WROW0 + 95) / 96);
  if (wave < 4) {
    const u32 fso = WS_FP + (wave * 64 + lane) * 8;
    flag_wait<SPSLEEP>(ws, fso, tag, 0);
  }
  BAR();
  STAMP(13);
  if (c == 31 && rb == 0 && threadIdx.x == 0) flag_st(ws, WS_EPOCH, tag);
  if (wave < 4) {
    const u32x4 srdp = mk_srd(ws, WS_END);
    const u32 lds0 = LDS0, l16 = lane * 16;
    const u32 psrc = WS_WDF + (cb2 * 54 + d_k0(wave) + d_step(4, d_rot())) * 5120;
    asm volatile("s_nop 4" ::: "memory");
#pragma unroll
    for (int i = 0; i < 20; i++) DMA16C(lds0 + p_off(i) + p_base(wave, i), l16, srdp, psrc + i * 1024);
  }
  STAMP(4);
}

DI void phase2(LAS char* smem, const u16* x, u16* out, int rb, int cb, int red, char* ws, u64 tag SP_ARG) {
  const int lane = threadIdx.x & 63;
  const int wave = __builtin_amdgcn_readfirstlane(threadIdx.x >> 6);
  const int w = wave & 3;
  const u32 l16 = lane * 16;
  const u32 lds0 = LDS0;
  const rsrc_t wsr = __builtin_amdgcn_make_buffer_rsrc(ws, 0, WS_END, 0x00020000);
  const f32x4 z4 = {0, 0, 0, 0};
  auto tile = [&](int i) { return (w + 1 + i) & 3; };
  u32x2 xr[3];
  u32 xo;
  auto ldx = [&](int n0, int n1) {
    u32 l = l16;
    asm volatile("" : "+v"(l));
    xo = (rb * 64 + w * 16 + ((l >> 4) & 15)) * D_ + cb * 80 + (l >> 8) * 4;
#pragma unroll
    for (int n = n0; n < n1; n++) xr[n - n0] = *(const u32x2*)(x + xo + n * 16);
  };
  auto dpart = [&](int k, int n) { const int f = (3 - k) * 5 + n; return *(LAS f32x4*)(smem + p_off(f) + p_base((w + k) & 3, f) + l16); };
  auto spart = [&](int k, int n) { return *(LAS f32x4*)(smem + L_R + ((w + k) & 3) * R_SZ + ((3 - k) * 5 + n - (k == 0 ? 3 : 0)) * 1024 + l16); };
  auto finish = [&](f32x4 bs, f32x4 ss, u32x2 xv, int n) {
    float r[4];
#pragma unroll
    for (int j = 0; j < 4; j++) {
      const float v = bs[j], gt = v * __builtin_amdgcn_rcpf(1.f + __builtin_amdgcn_exp2f(-1.44269504089f * v));
      r[j] = ((j & 1) ? bhi(xv[j >> 1]) : blo(xv[j >> 1])) + ss[j] * gt;
    }
    *(u32x2*)(out + xo + n * 16) = u32x2{pk2(r[0], r[1]), pk2(r[2], r[3])};
  };
  if (wave < 4) {
    const int k0 = d_k0(w);
    const int ha = WS_HF + (rb * 54 + k0) * 4096, wa = WS_WDF + (cb * 54 + k0) * 5120;
    const int rot = d_rot();
    auto vof = [&](int s) { return (s == 3 || s == 8 || s == 13) ? l16 + ((w >= 2 && s + rot == 13) ? OOB : 0) : l16; };
    u32x4 a[14][4], b[14][5];
    auto lda = [&](int s) {
#pragma unroll
      for (int t = 0; t < 4; t++) a[s][t] = LD128(wsr, vof(s), ha + d_step(s, rot) * 4096 + tile(t) * 1024, LDAUX);
    };
    auto ldb = [&](int s) {
#pragma unroll
      for (int n = 0; n < 5; n++) b[s][n] = LD128(wsr, vof(s) + (n & 3) * 1024, wa + d_step(s, rot) * 5120 + (n >> 2) * 4096, LWAUX);
    };
    auto ldl = [&](int s) {
#pragma unroll
      for (int n = 0; n < 5; n++) { const int f = (s - 4) * 5 + n; b[s][n] = *(LAS u32x4*)(smem + p_off(f) + p_base(w, f) + l16); }
    };
    __builtin_amdgcn_s_setprio(3);
#pragma unroll
    for (int s = 0; s < 4; s++) ldb(s);
    SCHED();
    if (red >= 0) {
      const int hg = red >> 1, q = (red & 1) * 216 + threadIdx.x;
      group_wait<2>(smem, L_CNT + 32, w == 0, ws, WS_FKV + (hg * 16 + (lane & 7)) * 8, tag);
      STAMP(3);
      f32x4 kp[8];
#pragma unroll
      for (int p = 0; p < 8; p++)
        kp[p] = __builtin_bit_cast(f32x4, LD128(wsr, threadIdx.x < 216 && q < 420 ? q * 16 : OOB, WS_KVP + (hg * 8 + p) * 6720, SC1));
      const f32x4 t = ((kp[0] + kp[1]) + (kp[2] + kp[3])) + ((kp[4] + kp[5]) + (kp[6] + kp[7]));
      ST64((u32x2{pk2(t[0], t[1]), pk2(t[2], t[3])}), wsr, threadIdx.x < 216 ? q * 8 : OOB, WS_KVC + hg * 3456);
      arrive(smem, L_CNT + 8, 4, ws, WS_FK + red * 8, tag, 32, 128);
      lds_wait(smem, L_CNT + 8, 4);
      STAMP(5);
    }
    group_wait<DSLEEP>(smem, L_CNT + 36, w == 0 && red < 0, ws, WS_FG + (rb * 32 + (lane < 17 ? lane : 17)) * 8, tag);
    STAMP(8);
    SCHED();
#pragma unroll
    for (int s = 0; s < 5; s++) lda(s);
    SCHED();
    __builtin_amdgcn_s_setprio(DP_RUN);
    f32x4 acc[4][5];
#pragma unroll
    for (int s = 0; s < 14; s++) {
      SCHED();
#pragma unroll
      for (int t = 0; t < 4; t++)
#pragma unroll
        for (int n = 0; n < 5; n++) {
          acc[t][n] = MFMA16(b[s][n], a[s][t], (s == 0 ? z4 : acc[t][n]));
        }
      if (s == 1) { lda(5); lda(6); }
      if (s == 2) { lda(7); }
      if (s == 3) { lda(8); }
      if (s == 4) { ldb(8); lda(9); }
      if (s == 5) { ldb(9); }
      if (s == 6) { lda(10); }
      if (s == 7) { ldb(10); }
      if (s == 8) { lda(11); ldb(11); }
      if (s == 9) { lda(12); ldb(12); }
      if (s == 10) { lda(13); ldb(13); }
      if (s == 2) ldl(4);
      if (s >= 4 && s <= 6) ldl(s + 1);
      if (s == 11) ldx(3, 5);
#pragma unroll
      for (int i = 0; i < 10; i++) {
        __builtin_amdgcn_sched_group_barrier(0x8, 2, 2);
        __builtin_amdgcn_sched_group_barrier(0x20, 1, 2);
        __builtin_amdgcn_sched_group_barrier(0x100, 1, 2);
      }
      SCHED();
      if (s == 0) STAMP(9);
    }
    STAMP(10);
#pragma unroll
    for (int f = 0; f < 18; f++) *(LAS f32x4*)(smem + p_off(f) + p_base(w, f) + l16) = acc[f / 5][f % 5];
    WAIT_L0(); BAR();
    STAMP(11);
#pragma unroll
    for (int n = 3; n < 5; n++) {
      const f32x4 bs = (acc[3][n] + dpart(1, n)) + (dpart(2, n) + dpart(3, n));
      const f32x4 ss = (spart(0, n) + spart(1, n)) + (spart(2, n) + spart(3, n));
      finish(bs, ss, xr[n - 3], n);
    }
  } else {
    const int g = lane >> 4, c15 = lane & 15;
    const u32x4 srdp = mk_srd(ws, WS_END);
    const u32 rl = lds0 + L_R + w * R_SZ;
    __builtin_amdgcn_s_setprio(SP_W);
    u32x4 wf[5][5], pq[4][2][2];
    u32x4 ph[4];
    const int wa = WS_WOF + (cb * 20 + w * 5) * 5120;
#pragma unroll
    for (int s = 0; s < 3; s++)
#pragma unroll
      for (int n = 0; n < 5; n++) wf[s][n] = LD128(wsr, l16 + (n & 3) * 1024, wa + s * 5120 + (n >> 2) * 4096, SC1);
    asm volatile("s_nop 4" ::: "memory");
#pragma unroll
    for (int i = 0; i < 10; i++) DMA16C(rl + i * 1024, l16, srdp, wa + 3 * 5120 + i * 1024);
    SCHED();
    group_wait(smem, L_CNT + 44, w == 0, ws, WS_FG + (rb * 32 + 26 + (lane & 3)) * 8, tag);
    STAMP(7);
#pragma unroll
    for (int h = 0; h < 2; h++)
#pragma unroll
      for (int t = 0; t < 4; t++) {
        const int qa = WS_QF + ((rb * 4 + tile(t)) * 8 + 2 * w + h) * 2560;
        pq[t][h][0] = LD128(wsr, l16, qa, LDAUX); pq[t][h][1] = LD128(wsr, l16 + 1024, qa, LDAUX);
        const u32x2 v = LD64(wsr, lane * 8 + 2048, qa, LDAUX);
        ph[t][2 * h] = v[0]; ph[t][2 * h + 1] = v[1];
      }
    SCHED();
    constexpr int ga[18][2] = {{0, 0}, {0, 1}, {1, 0}, {1, 1}, {1, 2}, {2, 1}, {2, 2}, {2, 3}, {3, 2}, {3, 3}, {3, 4}, {4, 3}, {4, 4}, {5, 0}, {5, 1}, {5, 2}, {5, 3}, {5, 4}};
    u32 gad[18];
#pragma unroll
    for (int k = 0; k < 18; k++) {
      const int j = ga[k][0], e = 16 * j + c15, hd = j == 5 ? (c15 & 3) : e / 20, row = j == 5 ? 20 : e - hd * 20;
      const u32 d = 16 * ga[k][1] + 4 * g - 20 * hd;
      gad[k] = rl + 10240 + (d < 20u ? hd * 840 + row * 40 + d * 2 : 3360);
      asm volatile("" : "+v"(gad[k]));
    }
    __builtin_amdgcn_s_setprio(SP_POLL);
    if (red >= 0 && w == 0) group_wait(smem, L_CNT + 36, true, ws, WS_FG + (rb * 32 + (lane < 17 ? lane : 17)) * 8, tag);
    group_wait(smem, L_CNT + 40, w == 0, ws, WS_FK + rb * 128 + (lane & 15) * 8, tag);
    __builtin_amdgcn_s_setprio(SP_HI);
    STAMP(8);
#pragma unroll
    for (int i = 0; i < 7; i++) DMA16C(rl + 10240 + i * 1024, l16, srdp, WS_KVC + w * 6912 + i * 1024);
    WAIT_V(0);
    STAMP(9);
    SCHED();
    {
      u32x2 gq[18];
      f32x4 on[2][6];
      const f32x4 e4 = {1e-6f, 1e-6f, 1e-6f, 1e-6f};
      auto fr2 = [&](int lo, int hi) { return u32x4{lo < 0 ? 0u : gq[lo][0], lo < 0 ? 0u : gq[lo][1], hi < 0 ? 0u : gq[hi][0], hi < 0 ? 0u : gq[hi][1]}; };
      auto mm = [&](int u) {
        const int h = u >> 2, t = u & 3;
        f32x4(&o)[6] = on[u & 1];
        const u32x4 p0 = pq[t][h][0], p1 = pq[t][h][1], p2 = ph[t];
        auto fr5 = [&](int k) { return h ? fr2(-1, k) : fr2(k, -1); };
        o[5] = MFMA16(fr2(13, 14), p0, e4);
        o[0] = MFMA16(fr2(0, 1), p0, z4);
        o[1] = MFMA16(fr2(2, 3), p0, z4);
        o[2] = MFMA16(fr2(-1, 5), p0, z4);
        o[5] = MFMA16(fr2(15, 16), p1, o[5]);
        o[3] = MFMA16(fr2(8, 9), p1, z4);
        o[4] = MFMA16(fr2(-1, 11), p1, z4);
        o[1] = MFMA16(fr2(4, -1), p1, o[1]);
        o[5] = MFMA16(fr5(17), p2, o[5]);
        o[2] = MFMA16(fr2(6, 7), p1, o[2]);
        o[3] = MFMA16(fr5(10), p2, o[3]);
        o[4] = MFMA16(fr5(12), p2, o[4]);
      };
      auto scale = [&](int u) {
        const int h = u >> 2, t = u & 3;
        f32x4(&o)[6] = on[u & 1];
        float rd[4];
#pragma unroll
        for (int j = 0; j < 4; j++) rd[j] = __builtin_amdgcn_rcpf(o[5][j]);
        const float rs[5] = {rd[0], g == 0 ? rd[0] : rd[1], g < 2 ? rd[1] : rd[2], g < 3 ? rd[2] : rd[3], rd[3]};
        u32 op[5][2];
#pragma unroll
        for (int j = 0; j < 5; j++) { op[j][0] = pk2(o[j][0] * rs[j], o[j][1] * rs[j]); op[j][1] = pk2(o[j][2] * rs[j], o[j][3] * rs[j]); }
        pq[t][h][0] = u32x4{op[0][0], op[0][1], op[1][0], op[1][1]};
        pq[t][h][1] = u32x4{op[2][0], op[2][1], op[3][0], op[3][1]};
        ph[t][2 * h] = op[4][0]; ph[t][2 * h + 1] = op[4][1];
      };
#pragma unroll
      for (int u = 0; u < 9; u++) {
        if ((u & 3) == 0 && u < 8) {
#pragma unroll
          for (int k = 0; k < 18; k++) gq[k] = *(LAS u32x2*)((LAS char*)(size_t)gad[k] + (u >> 2) * 3456);
          if (u > 0) { scale(u - 1); SCHED(); }
          mm(u);
        } else {
          if (u < 8) mm(u);
          scale(u - 1);
#pragma unroll
          for (int i = 0; i < 12; i++) { __builtin_amdgcn_sched_group_barrier(0x8, 1, 1); __builtin_amdgcn_sched_group_barrier(0x2, 3, 1); }
        }
        SCHED();
        if (u == 0) STAMP(13);
        if (u == 3) STAMP(14);
        if (u == 4) STAMP(5);
      }
    }
    STAMP(10);
#pragma unroll
    for (int i = 0; i < 10; i++) wf[3 + i / 5][i % 5] = *(LAS u32x4*)((LAS char*)(size_t)rl + i * 1024 + l16);
    ldx(0, 3);
    WAIT_L0();
    f32x4 acc[5];
#pragma unroll
    for (int t = 0; t < 4; t++) {
#pragma unroll
      for (int s = 0; s < 5; s++) {
        const u32x4 o = s < 4 ? pq[t][s >> 1][s & 1] : ph[t];
#pragma unroll
        for (int n = 0; n < 5; n++) acc[n] = MFMA16(wf[s][n], o, (s == 0 ? z4 : acc[n]));
      }
#pragma unroll
      for (int n = (t == 3 ? 3 : 0); n < 5; n++) *(LAS f32x4*)((LAS char*)(size_t)rl + (t * 5 + n - (t == 3 ? 3 : 0)) * 1024 + l16) = acc[n];
    }
    STAMP(12);
    WAIT_L0(); BAR();
    STAMP(11);
#pragma unroll
    for (int n = 0; n < 3; n++) {
      const f32x4 bs = (dpart(0, n) + dpart(1, n)) + (dpart(2, n) + dpart(3, n));
      const f32x4 ss = (acc[n] + spart(1, n)) + (spart(2, n) + spart(3, n));
      finish(bs, ss, xr[n], n);
    }
  }
}

__global__ __launch_bounds__(512, 2) void fused(const u16* __restrict__ x, const u16* __restrict__ wqkv, const u16* __restrict__ wo,
                                                const u16* __restrict__ wgt, const u16* __restrict__ wu, const u16* __restrict__ wd,
                                                u16* __restrict__ out, char* ws) {
  extern __shared__ __attribute__((aligned(16))) char smem_raw[];
  LAS char* smem = (LAS char*)smem_raw;
  STAMP_INIT(); STAMP(0);
  const int xcd = blockIdx.x & 7, idx = blockIdx.x >> 3;
  const int rb = (xcd >> 2) * 4 + (idx < 28 ? (idx & 3) : (xcd & 2) + (idx & 1));
  const int c = idx < 28 ? (idx >> 2) * 4 + (xcd & 3) : 28 + (idx & 2) + (xcd & 1);
  const int rb2 = xcd * 4 + (idx >> 3), cb2 = idx & 7;
  u64 tag;
  if (threadIdx.x < 16) ((LAS u32*)(smem + L_CNT))[threadIdx.x] = 0;
  if (__builtin_amdgcn_readfirstlane(threadIdx.x >> 6) == 7) {
    u64 pc; asm volatile("s_getpc_b64 %0" : "=s"(pc));
    const u32x4 srdc = mk_srd((const void*)pc, CODEWARM);
    const u32 o = (idx & 7) * 8192 + (threadIdx.x & 63) * 128;
    asm volatile("s_mov_b32 m0, %0\n\ts_nop 4\n\tbuffer_load_dword %1, %2, 0 offen lds" ::"s"(LDS0 + L_DUMMY), "v"(o), "s"(srdc) : "memory");
  }
  if (c >= 30) spare(smem, x, wqkv, wgt, wu, rb, c, cb2, ws, tag SP_PASS);
  else {
    const int lane = threadIdx.x & 63;
    const int wave = __builtin_amdgcn_readfirstlane(threadIdx.x >> 6);
    const int grp = wave >> 2, wg = wave & 3, img = wg >> 1;
    const u32 lds0 = LDS0;
    P1 p;
    const u32x4 srdx = mk_srd(x + (size_t)rb * 256 * D_, 256 * D_ * 2);
    u32 vx[2];
#pragma unroll
    for (int i = 0; i < 2; i++) { const int r = 8 * i + (lane >> 3); vx[i] = (64 * wg + r) * (D_ * 2) + (((lane & 7) ^ ((r >> 1) & 7)) << 4); }
    const int unit = ((c < 18 ? c : c >= 26 ? c - 8 : c + 4) * 8 + rb) * 4 + wg, uu = unit < 740 ? unit : 739, uks = uu / 10, ucg = uu - uks * 10;
    asm volatile("s_nop 4" ::: "memory");
#pragma unroll
    for (int i = 0; i < 8; i++) DMA16C(lds0 + grp * L_X1 + wg * 8192 + i * 1024, vx[i & 1], srdx, grp * 128 + (i >> 1) * 16 * D_ * 2);
    int col;
    if (c < 18) col = c * 96;
    else if (c < 26) col = 640 + img * 640 + 80 * (c - 18) - 16 * (c & 1);
    else col = 160 * (c - 26) + img * 64;
    p.pitch = c < 18 ? F_ * 2 : 3840;
    const u16* wb = (c < 18 ? (img ? wu : wgt) : wqkv) + col;
    const u32x4 srdw = mk_srd(wb, D_ * p.pitch - col * 2);
#pragma unroll
    for (int i = 0; i < 6; i++) { const int o = (6 * (wg & 1) + i) * 1024 + lane * 16, r = o / 192; p.vo[i] = r * p.pitch + ((o - r * 192) ^ ((r & 8) << 2)); }
    asm volatile("s_nop 4" ::: "memory");
#pragma unroll
    for (int i = 0; i < 6; i++) DMA16C(lds0 + (grp ? L_W1 : L_W0) + wg * 6144 + i * 1024, p.vo[i], srdw, grp * 64 * p.pitch);
    if (grp) {
      int r0, r1;
      if (uks < 54) { r0 = 32 * uks; r1 = r0 + 16; }
      else {
        const int k = uks - 54, pr = k / 5, s5 = k - pr * 5;
        if (s5 < 4) { r0 = 160 * pr + 80 * (s5 >> 1) + 32 * (s5 & 1); r1 = r0 + 16; } else { r0 = 160 * pr + 64; r1 = r0 + 80; }
      }
      const u32x4 srdu = uks < 54 ? mk_srd(wd, F_ * D_ * 2) : mk_srd(wo, unit < 740 ? D_ * D_ * 2 : 0);
      const u32 vu = (lane >> 3) * (D_ * 2) + (lane & 7) * 16;
      asm volatile("s_nop 4" ::: "memory");
#pragma unroll
      for (int i = 0; i < 4; i++) DMA16(lds0 + L_SP + wg * 4096 + i * 1024, vu, srdu, ((i < 2 ? r0 : r1) + 8 * (i & 1)) * (D_ * 2) + ucg * 128);
    }
    {
      const int g = lane >> 4;
      const bool alt = uks < 54 && uks % 3 == 1;
      p.ua = lds0 + L_SP + wg * 4096 + ((alt ? 16 * (g >> 1) + 4 * (g & 1) : 4 * g) + ((lane & 15) >> 2)) * 128 + (lane & 3) * 8;
      p.ub = p.ua + (alt ? 8 * 128 : 16 * 128);
#pragma unroll
      for (int j = 0; j < 4; j++) {
        const int n16 = ucg * 4 + j, cbk = n16 / 5, nt = n16 - cbk * 5;
        p.ud[j] = unit >= 740 ? OOB : (uks < 54 ? WS_WDF + (cbk * 54 + uks) * 5120 : WS_WOF + (cbk * 20 + uks - 54) * 5120) + nt * 1024;
      }
    }
    tag = get_tag(ws);
    if (grp == 0) {
      p.srd = srdx; p.vo[0] = vx[0]; p.vo[1] = vx[1];
    } else {
      p.srd = srdw;
    }
    STAMP(12);
    if (c < 18) phase1<0>(smem, p, 0, 0, rb, c, c, cb2, ws, tag SP_PASS);
    else if (c < 26) phase1<1>(smem, p, c & 1, c & 1, rb, c - 18, c, cb2, ws, tag SP_PASS);
    else phase1<2>(smem, p, 0, 1, rb, c - 26, c, cb2, ws, tag SP_PASS);
  }
  phase2(smem, x, out, rb2, cb2, c >= 30 ? rb * 2 + c - 30 : -1, ws, tag SP_PASS);
  STAMP(15); STAMP_FLUSH(ws);
}

extern "C" void kernel_launch(void* const* d_in, const int* in_sizes, int n_in, void* d_out, int out_size, void* d_ws, size_t ws_size,
                              hipStream_t stream) {
  (void)hipFuncSetAttribute((const void*)fused, hipFuncAttributeMaxDynamicSharedMemorySize, LDS_BYTES);
  fused<<<256, 512, LDS_BYTES, stream>>>((const u16*)d_in[0], (const u16*)d_in[1], (const u16*)d_in[2], (const u16*)d_in[4],
                                         (const u16*)d_in[5], (const u16*)d_in[6], (u16*)d_out, (char*)d_ws);
}
```

```cpp
#include <hip/hip_runtime.h>
#include <type_traits>
#define LATE (c >= 18 && c < 26)
#define EPRIO 1
#define DP_RUN 0
#define DP_POLL 3
#define GPOS 0
#define SP_W 2
#define SP_HI 2
#define SP_POLL 2
#define CODEWARM 52200
typedef unsigned short u16;
typedef unsigned int u32;
typedef unsigned long long u64;
typedef short s16x4 __attribute__((ext_vector_type(4)));
typedef short s16x8 __attribute__((ext_vector_type(8)));
typedef __bf16 bf16x8 __attribute__((ext_vector_type(8)));
typedef __bf16 bf16x2 __attribute__((ext_vector_type(2)));
typedef float f32x2 __attribute__((ext_vector_type(2)));
typedef float f32x4 __attribute__((ext_vector_type(4)));
typedef u32 u32x2 __attribute__((ext_vector_type(2)));
typedef u32 u32x4 __attribute__((ext_vector_type(4)));
#define LAS __attribute__((address_space(3)))
#define GAS __attribute__((address_space(1)))
#define DI __device__ __forceinline__
#define LDS0 ((u32)__builtin_amdgcn_readfirstlane((u32)(size_t)smem))

constexpr int S_ = 2048, D_ = 640, F_ = 1728;
constexpr int WS_EPOCH = 0;
constexpr int WS_FG   = 4096;
constexpr int WS_FKV  = WS_FG + 8192;
constexpr int WS_FP   = WS_FKV + 1024;
constexpr int WS_FK   = 24576;
constexpr int WS_STAT = 491520 + 16384;
constexpr int WS_KVC  = 32768;
constexpr int WS_KVP  = 65536;
constexpr int WS_HF   = 1 << 20;
constexpr int WS_QF   = WS_HF + 32 * 54 * 4096;
constexpr int WS_WDF  = WS_QF + 128 * 20 * 1024;
constexpr int WS_WOF  = WS_WDF + 8 * 54 * 5120;
constexpr int WS_END  = WS_WOF + 8 * 20 * 5120;
constexpr size_t WS_DBG = 64u << 20;
constexpr u64 TAG_STEP = 0x9E3779B97F4A7C15ull;
constexpr u32 SPIN_CAP = 1u << 16;
constexpr int SC1 = 16;
#define LDAUX 16
#define LWAUX 16
constexpr int OOB = 0x40000000;

constexpr int L_X0 = 0, L_W1 = 32768, L_W2 = 57344, L_X1 = 81920, L_W0 = 114688, L_SP = 139264;
constexpr int W_IMG1 = 12288;
constexpr int L_R = 81920, R_SZ = 19968;
constexpr int L_ST = 158976;
constexpr int L_DUMMY = 161792;
constexpr int L_CS = L_R + 17408;
constexpr int L_CNT = 163584;
constexpr int LDS_BYTES = 163840;
DI constexpr int p_off(int f) { return f < 8 ? L_X0 + f * 1024 : f < 14 ? L_W1 + (f - 8) * 1024 : L_W2 + (f - 14) * 1024; }
DI int p_base(int w, int f) { return w * (f < 8 ? 8192 : 6144); }

DI u32 pk2(float a, float b) { f32x2 v = {a, b}; return __builtin_bit_cast(u32, __builtin_convertvector(v, bf16x2)); }
DI float blo(u32 d) { return __uint_as_float(d << 16); }
DI float bhi(u32 d) { return __uint_as_float(d & 0xffff0000u); }
DI float rbf(float f) { return blo(pk2(f, f)); }
DI u32x4 mk_srd(const void* p, u32 bytes) {
  u64 a = (u64)p;
  u32x4 r;
  r[0] = __builtin_amdgcn_readfirstlane((u32)a); r[1] = __builtin_amdgcn_readfirstlane((u32)(a >> 32) & 0xffffu);
  r[2] = __builtin_amdgcn_readfirstlane(bytes); r[3] = 0x00020000u;
  return r;
}
#define UPOL "nt"
#define WPOL "sc1"
#define DMA16W(m0v, voff, srd, soff) \
  asm volatile("s_mov_b32 m0, %0\n\ts_nop 0\n\tbuffer_load_dwordx4 %1, %2, %3 offen " WPOL " lds" ::"s"(m0v), "v"(voff), "s"(srd), "s"(soff) : "memory")
#define DMA16(m0v, voff, srd, soff) \
  asm volatile("s_mov_b32 m0, %0\n\ts_nop 0\n\tbuffer_load_dwordx4 %1, %2, %3 offen " UPOL " lds" ::"s"(m0v), "v"(voff), "s"(srd), "s"(soff) : "memory")
#define DMA16C(m0v, voff, srd, soff) \
  asm volatile("s_mov_b32 m0, %0\n\ts_nop 0\n\tbuffer_load_dwordx4 %1, %2, %3 offen sc1 lds" ::"s"(m0v), "v"(voff), "s"(srd), "s"(soff) : "memory")
#define TOUCH(m0v, voff, srd, soff) \
  asm volatile("s_mov_b32 m0, %0\n\ts_nop 0\n\tbuffer_load_dword %1, %2, %3 offen sc1 lds" ::"s"(m0v), "v"(voff), "s"(srd), "s"(soff) : "memory")
#define WAIT_V(n) asm volatile("s_waitcnt vmcnt(%0)" :: "i"(n) : "memory")
#define WAIT_L0() do { asm volatile("" ::: "memory"); __builtin_amdgcn_s_waitcnt(0xC07F); asm volatile("" ::: "memory"); } while (0)
#define BAR() __builtin_amdgcn_s_barrier()
#define SCHED() __builtin_amdgcn_sched_barrier(0)
#define MFMA16(a, b, c) __builtin_amdgcn_mfma_f32_16x16x32_bf16(__builtin_bit_cast(bf16x8, a), __builtin_bit_cast(bf16x8, b), c, 0, 0, 0)
#define LD128(r, v, s, aux) __builtin_amdgcn_raw_buffer_load_b128(r, v, s, aux)
#define LD64(r, v, s, aux) __builtin_amdgcn_raw_buffer_load_b64(r, v, s, aux)
#define ST128(x, r, v, s) __builtin_amdgcn_raw_buffer_store_b128(x, r, (v) + (s), 0, SC1)
#define ST64(x, r, v, s) __builtin_amdgcn_raw_buffer_store_b64(x, r, (v) + (s), 0, SC1)
typedef __amdgpu_buffer_rsrc_t rsrc_t;

#define STAMP_INIT() do {} while (0)
#define STAMP(i) do {} while (0)
#define IVSTAMP(t) do {} while (0)
#define STAMP_FLUSH(ws) do {} while (0)
#define TLS() do {} while (0)
#define SP_ARG
#define SP_PASS

DI u64 flag_ld(char* ws, u32 off) { return __hip_atomic_load((GAS u64*)(ws + off), __ATOMIC_RELAXED, __HIP_MEMORY_SCOPE_AGENT); }
DI void flag_st(char* ws, u32 off, u64 v) { __hip_atomic_store((GAS u64*)(ws + off), v, __ATOMIC_RELAXED, __HIP_MEMORY_SCOPE_AGENT); }
#define TMO_HIT(i, cond) do {} while (0)
template <int SLEEP = 2>
DI void flag_wait(char* ws, u32 off, u64 tag, u64 v) {
#pragma unroll 1
  for (u32 spins = 0; !__all(v == tag) && spins < SPIN_CAP; spins++) {
    __builtin_amdgcn_s_sleep(SLEEP);
    v = flag_ld(ws, off);
    __builtin_amdgcn_s_waitcnt(0x0F70);
    TMO_HIT(0, spins == SPIN_CAP - 1);
  }
  __builtin_amdgcn_fence(__ATOMIC_ACQUIRE, "wavefront");
}
DI void lds_wait(LAS char* smem, int off, u32 want) {
#pragma unroll 1
  for (u32 spins = 0; spins < SPIN_CAP; spins++) {
    const u32 v = __hip_atomic_load((LAS u32*)(smem + off), __ATOMIC_RELAXED, __HIP_MEMORY_SCOPE_WORKGROUP);
    if (__builtin_amdgcn_readfirstlane(v) >= want) break;
    __builtin_amdgcn_s_sleep(1);
    TMO_HIT(1, spins == SPIN_CAP - 1);
  }
  asm volatile("" ::: "memory");
}
#define SPSLEEP 48
#define NST 2
#define USKIP 0
#define ULEFT 2
#define UWAIT 6
#define SPOLL 64
#define AB_U 1
#define SSLEEP 5
#define DSLEEP 2
#define PSLEEP 6
typedef u64 u64x8 __attribute__((ext_vector_type(8)));
typedef u64 u64x4 __attribute__((ext_vector_type(4)));
typedef u64 u64x2 __attribute__((ext_vector_type(2)));
template <int N>
DI bool flags_set_scalar(char* p, u64 tag) {
  u64 bad = 0;
  if (N == 4) {
    u64x4 a;
    asm volatile("s_load_dwordx8 %0, %1, 0x0 glc\n\ts_waitcnt lgkmcnt(0)" : "=&s"(a) : "s"(p) : "memory");
#pragma unroll
    for (int i = 0; i < 4; i++) bad |= a[i] ^ tag;
  } else if (N == 8) {
    u64x8 a;
    asm volatile("s_load_dwordx16 %0, %1, 0x0 glc\n\ts_waitcnt lgkmcnt(0)" : "=&s"(a) : "s"(p) : "memory");
#pragma unroll
    for (int i = 0; i < 8; i++) bad |= a[i] ^ tag;
  } else {
    u64x8 a, b; u64x2 c;
    if (N == 16) {
      asm volatile("s_load_dwordx16 %0, %2, 0x0 glc\n\ts_load_dwordx16 %1, %2, 0x40 glc\n\ts_waitcnt lgkmcnt(0)" : "=&s"(a), "=&s"(b) : "s"(p) : "memory");
    } else {
      asm volatile("s_load_dwordx16 %0, %3, 0x0 glc\n\ts_load_dwordx16 %1, %3, 0x40 glc\n\ts_load_dwordx4 %2, %3, 0x80 glc\n\ts_waitcnt lgkmcnt(0)"
                   : "=&s"(a), "=&s"(b), "=&s"(c) : "s"(p) : "memory");
      bad = (c[0] ^ tag) | (c[1] ^ tag);
    }
#pragma unroll
    for (int i = 0; i < 8; i++) bad |= (a[i] ^ tag) | (b[i] ^ tag);
  }
  return bad == 0;
}
template <int N, int SLEEP = PSLEEP>
DI void group_wait(LAS char* smem, int word, bool leader, char* ws, u32 off, u64 tag) {
  if (leader) {
    const int lane = threadIdx.x & 63;
    const u32 vo = off + (lane < N ? lane : N - 1) * 8;
    bool ok = false;
#pragma unroll 1
    for (int i = 0; i < SPOLL && !(ok = flags_set_scalar<N>(ws + off, tag)); i++) __builtin_amdgcn_s_sleep(SSLEEP);
    if (!ok)
      flag_wait<SLEEP>(ws, vo, tag, flag_ld(ws, vo));
    *(volatile LAS u32*)(smem + word) = 1;
  } else lds_wait(smem, word, 1);
}
DI void arrive(LAS char* smem, int cnt, u32 n, char* ws, u32 flag, u64 tag, int rep = 1, int stride = 0) {
  WAIT_V(0);
  const int lane = threadIdx.x & 63;
  const u32 k = __hip_atomic_fetch_add((LAS u32*)(smem + (lane ? L_DUMMY + lane * 4 : cnt)), 1u, __ATOMIC_RELAXED, __HIP_MEMORY_SCOPE_WORKGROUP);
  const bool go = __builtin_amdgcn_readfirstlane(k) == n - 1 && lane < rep;
  const rsrc_t wsr = __builtin_amdgcn_make_buffer_rsrc(ws, 0, WS_END, 0x00020000);
  ST64((u32x2{(u32)tag, (u32)(tag >> 32)}), wsr, go ? flag + lane * stride : OOB, 0);
}
DI u64 get_tag(char* ws) {
  u64 e;
  asm volatile("s_load_dwordx2 %0, %1, 0x0\n\ts_waitcnt lgkmcnt(0)" : "=s"(e) : "s"(ws + WS_EPOCH) : "memory");
  return e + TAG_STEP;
}
DI int d_k0(int w) { return w < 2 ? w * 14 : 2 + w * 13; }
DI int d_rot() {
  const int idx = blockIdx.x >> 3;
  return (((idx & 7) + (idx >> 3)) % 3) * 5;
}
DI int d_step(int s, int r) { return s + r >= 14 ? s + r - 14 : s + r; }
struct P1 {
  u32x4 srd;
  u32 vo[6];
  u32 ua, ub, ud[4];
  int pitch;
};

template <int TYPE>
DI void phase1(LAS char* smem, P1& p, int o0, int o1, int rb, int cb, int c, int cb2, char* ws, u64 tag SP_ARG) {
  constexpr int NT = TYPE == 0 ? 6 : 5;
  const int lane = threadIdx.x & 63;
  const int wave = __builtin_amdgcn_readfirstlane(threadIdx.x >> 6);
  const int grp = wave >> 2, wg = wave & 3, wm = wave >> 1, wn = wave & 1;
  const u32 lds0 = LDS0;
  const u32 mb = lds0 + wg * (grp == 0 ? 8192 : 6144);
  const int g = lane >> 4, c15 = lane & 15;
  const u32 l16 = lane * 16;
  constexpr int NB = TYPE == 1 ? 5 : TYPE == 0 ? 3 : 2;
  u32 ax[2], aw[NB];
  const int ksw = wm & 1;
  ax[0] = lds0 + (wm * 64 + c15) * 128 + (((4 * ksw + g) ^ (c15 >> 1)) << 4);
  ax[1] = ax[0] ^ 64;
  {
    const u32 awr = lds0 + (8 * g + (c15 >> 2)) * 192, sw = (g & 1) << 5, p8 = (lane & 3) * 8;
#pragma unroll
    for (int n = 0; n < NB; n++) {
      if (TYPE == 0) aw[n] = awr + (((n < 2 ? 4 * wn + n : 2 + wn) * 32 + p8) ^ sw);
      else if (TYPE == 2) aw[n] = awr + wn * W_IMG1 + ((((wn ? o1 : o0) + n) * 32 + p8) ^ sw);
      else {
        const int j = 32 * n + p8;
        aw[n] = awr + (j < 80 ? 0 : W_IMG1) + ((32 * o0 + 80 * wn + (j < 80 ? j : j - 80)) ^ sw);
      }
    }
  }
  f32x4 acc[4][NT];
#pragma unroll
  for (int m = 0; m < 4; m++)
#pragma unroll
    for (int n = 0; n < NT; n++) acc[m][n] = f32x4{0, 0, 0, 0};
  s16x8 xf[2][4], wf[2][NT];
  const rsrc_t wsr = __builtin_amdgcn_make_buffer_rsrc(ws, 0, WS_END, 0x00020000);
  const u32x4 srdp = mk_srd(ws, WS_END);
  const u32 psrc = WS_WDF + (cb2 * 54 + d_k0(wg) + d_step(4, d_rot())) * 5120;
  u32 sr0 = __builtin_amdgcn_readfirstlane(p.srd[0]), sr1 = __builtin_amdgcn_readfirstlane(p.srd[1]), sr2 = __builtin_amdgcn_readfirstlane(p.srd[2]);
  u32 xstep = 16 * D_ * 2;

  auto trd = [&](LAS char* a) { return __builtin_amdgcn_ds_read_tr16_b64_v4i16((LAS s16x4*)a); };
  auto dma_x = [&](int i, u32 m, u32 so) { const u32x4 srd = {sr0, sr1, sr2, 0x00020000u}; DMA16C(m + i * 1024, p.vo[i & 1], srd, so + (i >> 1) * xstep); };
  auto dma_w = [&](int i, u32 m, u32 so) { const u32x4 srd = {sr0, sr1, sr2, 0x00020000u}; DMA16W(m + i * 1024, p.vo[i], srd, so); };
  u32 bw[NB], bx;
  auto load_q = [&](auto mode, int k2, int q, int d0, int dn, u32 xo, u32 wo, u32 m, u32 so) {
    constexpr int MODE = decltype(mode)::value;
    if (q == 0) {
      const u32 wk = wo + (k2 ^ ksw) * (32 * 192);
      bx = ax[k2] + xo;
#pragma unroll
      for (int n = 0; n < NB; n++) { bw[n] = aw[n] + wk; asm volatile("" : "+v"(bw[n])); }
      asm volatile("" : "+v"(bx));
    }
#pragma unroll
    for (int i = d0 + q * dn; i < d0 + (q + 1) * dn; i++) {
      if (MODE == 1 && i >= 0 && i < 8) dma_x(i, m, so);
      if (MODE == 2 && i >= 0 && i < 6) dma_w(i, m, so);
    }
    if (q == 0) {
#pragma unroll
      for (int mm = 0; mm < 4; mm++) xf[k2][mm] = *(LAS s16x8*)((LAS char*)(size_t)bx + mm * 2048);
    } else {
#pragma unroll
      for (int n = 2 * q - 2; n < 2 * q && n < NT; n++) {
        LAS char* a = (LAS char*)(size_t)bw[n % NB] + (TYPE == 0 ? (n / 3) * W_IMG1 : TYPE == 2 ? (n / 2) * 64 : 0);
        s16x4 lo = trd(a), hi = trd(a + 4 * 192);
        wf[k2][n] = __builtin_shufflevector(lo, hi, 0, 1, 2, 3, 4, 5, 6, 7);
      }
    }
  };
  auto load_half = [&](auto mode, int k2, int d0, int dn, u32 xo, u32 wo, u32 m, u32 so) {
#pragma unroll
    for (int q = 0; q < 4; q++) load_q(mode, k2, q, d0, dn, xo, wo, m, so);
  };
  auto load_phase = [&](auto mode, u32 xo, u32 wo, u32 m, u32 so) {
    constexpr int MODE = decltype(mode)::value;
    load_half(mode, 0, MODE == 1 ? 0 : -1, 1, xo, wo, m, so);
    load_half(mode, 1, MODE == 1 ? 4 : 3, 1, xo, wo, m, so);
  };
  auto comp_q = [&](int k2, int m) {
#pragma unroll
    for (int n = 0; n < NT; n++)
      acc[m][n] = TYPE == 1 ? MFMA16(xf[k2][m], wf[k2][n], acc[m][n]) : MFMA16(wf[k2][n], xf[k2][m], acc[m][n]);
  };
  auto comp_half = [&](int k2) {
#pragma unroll
    for (int m = 0; m < 4; m++) comp_q(k2, m);
  };
  auto compute = [&]() { comp_half(0); comp_half(1); };
  auto mixed = [&](int kc, u32 xo, u32 wo) {
#pragma unroll
    for (int q = 0; q < 4; q++) { load_q(std::integral_constant<int, 0>{}, 1 - kc, q, 0, 0, xo, wo, 0, 0); SCHED(); comp_q(kc, q); SCHED(); }
  };
  f32x4 cm[NT];
#pragma unroll
  for (int n = 0; n < NT; n++) cm[n] = f32x4{0, 0, 0, 0};
  const u32x4 ones = {0x3f803f80u, 0x3f803f80u, 0x3f803f80u, 0x3f803f80u};
  auto colsum = [&]() {
#pragma unroll
    for (int n = 0; n < NT; n++) cm[n] = MFMA16(ones, wf[0][n], cm[n]);
  };
  auto wait_tile0 = [&]() {
    const u32 t32 = (u32)tag;
#pragma unroll 1
    for (u32 spins = 0; spins < (SPIN_CAP << 4); spins++) {
      const u32x4 v = *(volatile LAS u32x4*)(smem + L_CNT + 64);
      if (__all(v[0] == t32 && v[1] == t32 && v[2] == t32 && v[3] == t32)) break;
      __builtin_amdgcn_s_sleep(1);
      TMO_HIT(2, spins == (SPIN_CAP << 4) - 1);
    }
    asm volatile("" ::: "memory");
  };
  using I0 = std::integral_constant<int, 0>; using I1 = std::integral_constant<int, 1>; using I2 = std::integral_constant<int, 2>;
  asm volatile("" : "+v"(ax[0]), "+v"(ax[1]));
#pragma unroll
  for (int n = 0; n < NB; n++) asm volatile("" : "+v"(aw[n]));
  int tl_ = 0; (void)tl_;
  if (grp == 0) {
    WAIT_V(0);
    *(volatile LAS u32*)(smem + L_CNT + 64 + wg * 4) = (u32)tag;
    wait_tile0();
    STAMP(1);
    SCHED();
    load_phase(I0{}, L_X0, L_W0, 0, 0); SCHED(); colsum(); compute(); SCHED();
    BAR(); SCHED();
    STAMP(6);
    u32 w_cur = L_W1, w_nxt = L_W2, w_prv = L_W0, so = 2 * 128, xo = L_X1;
    int left = 8;
#pragma unroll 1
    for (int seg = 0;; seg++) {
#pragma unroll 1
      for (;;) {
        TLS();
        load_phase(I1{}, xo, w_cur, mb + (L_X1 - xo), so);
        { const u32 u = w_prv; w_prv = w_cur; w_cur = w_nxt; w_nxt = u; }
        so += 128; xo = L_X1 - xo;
        TLS();
        SCHED(); colsum(); compute(); SCHED();
        TLS();
        if (--left == 0) break;
        WAIT_V(0); BAR(); SCHED();
      }
      if (seg) break;
      sr0 = srdp[0]; sr1 = srdp[1]; sr2 = srdp[2]; so = psrc; xstep = 2048; p.vo[0] = l16; p.vo[1] = l16 + 1024; left = 1;
      STAMP(14);
      WAIT_V(0); BAR(); SCHED();
    }
    BAR(); SCHED();
    if (lane < 16) {
#pragma unroll
      for (int n = 0; n < NT; n++) ((LAS float*)(smem + L_CS))[((ksw * 2 + wn) * 6 + n) * 16 + lane] = cm[n][0];
    }
    WAIT_L0();
    if (lane == 0) __hip_atomic_fetch_add((LAS u32*)(smem + L_CNT + 4), 1u, __ATOMIC_RELAXED, __HIP_MEMORY_SCOPE_WORKGROUP);
#pragma unroll
    for (int i = 0; i < 10; i++) DMA16C(lds0 + L_R + wg * R_SZ + i * 1024, l16, srdp, WS_WOF + (cb2 * 20 + wg * 5 + 3) * 5120 + i * 1024);
  } else {
    __builtin_amdgcn_s_setprio(1);
#pragma unroll
    for (int m = 0; m < 4; m++)
#pragma unroll
      for (int n = 0; n < NT; n++) asm volatile("" : "+v"(acc[m][n]));
    wait_tile0();
    STAMP(1);
    SCHED();
    u32 w_cur = L_W0, w_nxt = L_W1, w_prv = L_W2, so = 2 * 64 * p.pitch, sstep = 64 * p.pitch, xo = L_X0;
    const u32 fso = WS_FP + l16;
    u32x4 fa = {0, 0, 0, 0}, fb = fa;
    {
#define UL 0
      u32 uq[8] = {(u32)OOB, (u32)OOB, UL ? (u32)OOB : p.ud[0], UL ? (u32)OOB : p.ud[1], p.ud[UL ? 0 : 2], p.ud[UL ? 1 : 3], p.ud[2], p.ud[3]};
      p.ua -= 64 + UL * 64; p.ub -= 64 + UL * 64;
#pragma unroll 1
      for (int t = 0; t < 3 + UL; t++) {
        load_phase(I2{}, xo, w_cur, mb + w_prv, so);
        { const u32 u = w_prv; w_prv = w_cur; w_cur = w_nxt; w_nxt = u; }
        so += sstep; xo = L_X1 - xo;
#pragma unroll
        for (int j = 0; j < 2; j++) {
          const s16x4 lo = trd((LAS char*)(size_t)p.ua + j * 32), hi = trd((LAS char*)(size_t)p.ub + j * 32);
          ST128(__builtin_bit_cast(u32x4, __builtin_shufflevector(lo, hi, 0, 1, 2, 3, 4, 5, 6, 7)), wsr, l16, uq[j]);
        }
        p.ua += 64; p.ub += 64;
#pragma unroll
        for (int j = 0; j < 4 + 2 * UL; j++) uq[j] = uq[j + 2];
        WAIT_L0();
        if (UL && t == 0) WAIT_V(12); else WAIT_V(8);
        SCHED(); BAR(); SCHED();
        compute(); SCHED();
      }
    }
    arrive(smem, L_CNT + 12, 4, ws, WS_FP + (c * 8 + rb) * 8, tag);
    int left = 4 - UL;
#pragma unroll 1
    for (int seg = 1;; seg++) {
#pragma unroll 1
      do {
        load_phase(I2{}, xo, w_cur, mb + w_prv, so);
        { const u32 u = w_prv; w_prv = w_cur; w_cur = w_nxt; w_nxt = u; }
        so += sstep; xo = L_X1 - xo;
        WAIT_L0();
        asm volatile("s_waitcnt vmcnt(6)" : "+v"(fa), "+v"(fb) :: "memory");
        SCHED(); BAR(); SCHED();
        compute(); SCHED();
      } while (--left);
      if (seg == 3) break;
      left = seg;
      if (seg == 1)
        asm volatile("global_load_dwordx4 %0, %2, %3 sc1\n\tglobal_load_dwordx4 %1, %2, %3 offset:1024 sc1" : "+v"(fa), "+v"(fb) : "v"(fso), "s"(ws) : "memory");
      if (seg == 2) {
        const u32 t0 = (u32)tag, t1 = (u32)(tag >> 32);
        const u32 bad = ((fa[0] ^ t0) | (fa[1] ^ t1) | (fa[2] ^ t0) | (fa[3] ^ t1)) | ((fb[0] ^ t0) | (fb[1] ^ t1) | (fb[2] ^ t0) | (fb[3] ^ t1));
        if (__builtin_amdgcn_ballot_w64(bad != 0) != 0) {
#pragma unroll 1
          for (int i = 0; i < 4; i++) flag_wait<4>(ws, WS_FP + i * 512 + lane * 8, tag, flag_ld(ws, WS_FP + i * 512 + lane * 8));
          __builtin_amdgcn_s_waitcnt(0x0F70);
        }
        sr0 = srdp[0]; sr1 = srdp[1]; sr2 = srdp[2]; so = psrc + 8192; sstep = 6144;
        DMA16C(lds0 + L_ST + (wg & 1) * 1024, l16, srdp, WS_STAT + rb * 2048 + (wg & 1) * 1024);
#pragma unroll
        for (int i = 0; i < 6; i++) p.vo[i] = l16 + i * 1024;
      }
      SCHED();
    }
    __builtin_amdgcn_s_setprio(0);
  }
  STAMP(2);

  lds_wait(smem, L_CNT + 4, 4);
  LAS float* csl = (LAS float*)(smem + L_CS) + wn * 96;
  LAS float* stl = (LAS float*)(smem + L_ST) + wm * 64;
  auto lo2 = [](f32x4 v) { return f32x2{v[0], v[1]}; };
  auto hi2 = [](f32x4 v) { return f32x2{v[2], v[3]}; };
  auto fma2 = [](f32x2 a, f32x2 b, f32x2 c) { return __builtin_elementwise_fma(a, b, c); };
  auto pkv = [](f32x2 v) { return __builtin_bit_cast(u32, __builtin_convertvector(v, bf16x2)); };
  auto phi2 = [&](f32x2 t, f32x2 rs, f32x2 rl, float c1 = 1.f + 1e-6f, float c2 = 1e-6f) {
    const f32x2 u = t * rl, lin = fma2(t, rs, f32x2{c1, c1});
    const f32x2 e = f32x2{__builtin_amdgcn_fmed3f(__builtin_amdgcn_exp2f(u[0]), 0.f, 1.f), __builtin_amdgcn_fmed3f(__builtin_amdgcn_exp2f(u[1]), 0.f, 1.f)} + f32x2{c2, c2};
    return f32x2{fmaxf(lin[0], e[0]), fmaxf(lin[1], e[1])};
  };
  if (TYPE != 1) {
    f32x4 cv[NT];
#pragma unroll
    for (int n = 0; n < NT; n++) cv[n] = *(LAS f32x4*)(csl + n * 16 + g * 4) + *(LAS f32x4*)(csl + 192 + n * 16 + g * 4);
    u32 odd[4][2];
#pragma unroll
    for (int m = 0; m < 4; m++) {
      const float mu1 = stl[m * 16 + c15], r = stl[256 + m * 16 + c15];
      const f32x2 mu = {mu1, mu1};
      u32 o[5][2];
      if (TYPE == 0) {
        const f32x2 r2 = {r * r, r * r};
#pragma unroll
        for (int n = 0; n < 3; n++)
#pragma unroll
          for (int h = 0; h < 2; h++) {
            const f32x2 gt = fma2(mu, h ? hi2(cv[n]) : lo2(cv[n]), h ? hi2(acc[m][n]) : lo2(acc[m][n]));
            const f32x2 up = fma2(mu, h ? hi2(cv[(n + 3) % NT]) : lo2(cv[(n + 3) % NT]), h ? hi2(acc[m][(n + 3) % NT]) : lo2(acc[m][(n + 3) % NT]));
            o[n][h] = pkv(f32x2{fmaxf(gt[0], 0.f), fmaxf(gt[1], 0.f)} * (up * r2));
          }
        const int base = WS_HF + ((rb * 4 + wm) * 54 + cb * 3) * 4096;
        ST128((u32x4{o[0][0], o[0][1], o[1][0], o[1][1]}), wsr, l16 + m * 1024, base + wn * 8192);
        odd[m][0] = o[2][0]; odd[m][1] = o[2][1];
        if (m & 1) {
          const auto s0 = __builtin_amdgcn_permlane32_swap(odd[m - 1][0], odd[m][0], false, false);
          const auto s1 = __builtin_amdgcn_permlane32_swap(odd[m - 1][1], odd[m][1], false, false);
          ST128((u32x4{s0[0], s1[0], s0[1], s1[1]}), wsr, (lane >> 5) * 1024 + (lane & 31) * 16 + (m >> 1) * 2048, base + 4096 + wn * 512);
        }
      } else {
        const f32x2 rs = {r, r}, rl = {r * 1.44269504089f, r * 1.44269504089f};
#pragma unroll
        for (int n = 0; n < 5; n++) {
          o[n][0] = pkv(phi2(fma2(mu, lo2(cv[n]), lo2(acc[m][n])), rs, rl));
          o[n][1] = pkv(phi2(fma2(mu, hi2(cv[n]), hi2(acc[m][n])), rs, rl));
        }
        const int base = WS_QF + ((rb * 16 + wm * 4 + m) * 8 + cb * 2 + wn) * 2560;
        ST128((u32x4{o[0][0], o[0][1], o[1][0], o[1][1]}), wsr, l16, base);
        ST128((u32x4{o[2][0], o[2][1], o[3][0], o[3][1]}), wsr, l16 + 1024, base);
        ST64((u32x2{o[4][0], o[4][1]}), wsr, lane * 8 + 2048, base);
      }
    }
  } else {
    f32x2 cv[5];
#pragma unroll
    for (int n = 0; n < 5; n++) { const float v = csl[n * 16 + c15] + csl[192 + n * 16 + c15]; cv[n] = f32x2{v, v}; }
    const float mc1 = c15 < 8 ? 1.f + 1e-6f : 0.f, mc2 = c15 < 8 ? 1e-6f : -__builtin_inff();
    f32x4 kv[7], kz[3];
    constexpr int ja[7] = {0, 0, 1, 1, 1, 2, 2}, jb[7] = {2, 3, 2, 3, 4, 3, 4};
#pragma unroll
    for (int mp = 0; mp < 2; mp++) {
      u32x4 fr[5];
#pragma unroll
      for (int mm = 0; mm < 2; mm++) {
        const int m = 2 * mp + mm;
        const f32x4 mu4 = *(LAS f32x4*)(stl + m * 16 + g * 4), rs4 = *(LAS f32x4*)(stl + 256 + m * 16 + g * 4);
#pragma unroll
        for (int h = 0; h < 2; h++) {
          const f32x2 mu = h ? hi2(mu4) : lo2(mu4), rs = h ? hi2(rs4) : lo2(rs4);
          const f32x2 rl = rs * f32x2{1.44269504089f, 1.44269504089f};
#pragma unroll
          for (int n = 0; n < 5; n++) {
            const f32x2 t = fma2(mu, cv[n], h ? hi2(acc[m][n]) : lo2(acc[m][n]));
            fr[n][2 * mm + h] = n < 2 ? pkv(phi2(t, rs, rl)) : n > 2 ? pkv(t * rs) : pkv(phi2(t, rs, rl, mc1, mc2));
          }
        }
      }
#pragma unroll
      for (int i = 0; i < 7; i++) kv[i] = MFMA16(fr[ja[i]], fr[jb[i]], (mp == 0 ? f32x4{0, 0, 0, 0} : kv[i]));
#pragma unroll
      for (int i = 0; i < 3; i++) kz[i] = MFMA16(fr[i], ones, (mp == 0 ? f32x4{0, 0, 0, 0} : kz[i]));
    }
    int ob[3], eb[3];
#pragma unroll
    for (int i = 0; i < 3; i++) {
      const int d = 16 * i + 4 * g, hl = d >= 20;
      ob[i] = L_R + 10240 + wm * R_SZ + (2 * wn + hl) * 1680 + (d - 20 * hl) * 4;
      eb[i] = d < 40 ? c15 - 40 - 20 * hl : 1000;
    }
#pragma unroll
    for (int i = 0; i < 7; i++) {
      const u32 e = eb[ja[i]] + 16 * jb[i];
      if (e < 20u) *(LAS f32x4*)(smem + ob[ja[i]] + e * 80) = kv[i];
    }
#pragma unroll
    for (int i = 0; i < 3; i++)
      if (c15 == 0 && eb[i] < 0) *(LAS f32x4*)(smem + ob[i] + 1600) = kz[i];
    WAIT_L0(); STAMP(5); BAR();
    if (threadIdx.x < 420) {
      LAS char* q = smem + L_R + 10240 + threadIdx.x * 16;
      const f32x4 t = (*(LAS f32x4*)q + *(LAS f32x4*)(q + R_SZ)) + (*(LAS f32x4*)(q + 2 * R_SZ) + *(LAS f32x4*)(q + 3 * R_SZ));
      ST128(__builtin_bit_cast(u32x4, t), wsr, threadIdx.x * 16, WS_KVP + (cb * 8 + rb) * 6720);
    }
  }
  STAMP(3);
  arrive(smem, L_CNT, 8, ws, TYPE == 1 ? WS_FKV + (cb * 16 + rb) * 8 : WS_FG + (rb * 128 + c) * 8, tag, TYPE == 1 ? 1 : 4, 256);
  STAMP(4);
  lds_wait(smem, L_CNT, 8);
}

DI void spare(LAS char* smem, const u16* x, const u16* wqkv, const u16* wgt, const u16* wu, int rb, int c, int cb2, char* ws, u64& tag SP_ARG) {
  const int lane = threadIdx.x & 63;
  const int wave = __builtin_amdgcn_readfirstlane(threadIdx.x >> 6);
  const int g = lane >> 4, c15 = lane & 15;
  const rsrc_t wsr = __builtin_amdgcn_make_buffer_rsrc(ws, 0, WS_END, 0x00020000);
#define WROW0 352
#define WPRE0 192
#define WPREN 0
  const int gw = ((c - 30) * 8 + rb) * 8 + wave, mat = gw < 45 ? 0 : gw < 86 ? 1 : 2;
  const int pitch = mat ? F_ * 2 : 3840;
  const u32x4 sr = mk_srd(mat == 0 ? wqkv : mat == 1 ? wgt : wu, D_ * pitch);
  auto wtouch = [&](int r0, int n) {
    const u32 v = (gw - (mat == 0 ? 0 : mat == 1 ? 45 : 86)) * 8192 + lane * 128 + r0 * pitch;
    asm volatile("s_nop 4" ::: "memory");
#pragma unroll
    for (int j = 0; j < n; j++) TOUCH(LDS0 + L_DUMMY, v + j * 96 * pitch, sr, 0);
  };
  wtouch(WPRE0, WPREN);
#define QPRE 160
  {
    const u32x4 sq = mk_srd(wqkv, D_ * 3840);
    asm volatile("s_nop 4" ::: "memory");
    TOUCH(LDS0 + L_DUMMY, (u32)(QPRE * 3840 + gw * 8192 + lane * 128), sq, 0);
  }
  u32x4 xv[4][5];
  const int row0 = rb * 256 + (c - 30) * 128 + wave * 16;
#pragma unroll
  for (int p = 0; p < 4; p++)
#pragma unroll
    for (int i = 0; i < 5; i++) xv[p][i] = *(const u32x4*)(x + (row0 + 4 * p + g) * D_ + (c15 + 16 * i) * 8);
  SCHED();
  tag = get_tag(ws);
  auto row_sum = [](float v) {
    v += __builtin_bit_cast(float, __builtin_amdgcn_mov_dpp(__builtin_bit_cast(int, v), 0xB1, 0xF, 0xF, true));
    v += __builtin_bit_cast(float, __builtin_amdgcn_mov_dpp(__builtin_bit_cast(int, v), 0x4E, 0xF, 0xF, true));
    v += __builtin_bit_cast(float, __builtin_amdgcn_mov_dpp(__builtin_bit_cast(int, v), 0x141, 0xF, 0xF, true));
    v += __builtin_bit_cast(float, __builtin_amdgcn_mov_dpp(__builtin_bit_cast(int, v), 0x140, 0xF, 0xF, true));
    return v;
  };
#pragma unroll
  for (int p = 0; p < 4; p++) {
    float f[40], s0 = 0, s1 = 0;
#pragma unroll
    for (int i = 0; i < 20; i++) { f[2 * i] = blo(xv[p][i >> 2][i & 3]); f[2 * i + 1] = bhi(xv[p][i >> 2][i & 3]); s0 += f[2 * i]; s1 += f[2 * i + 1]; }
    const float m = row_sum(s0 + s1) * (1.f / D_);
    float q0 = 0, q1 = 0;
#pragma unroll
    for (int i = 0; i < 20; i++) { const float a = f[2 * i] - m, b = f[2 * i + 1] - m; q0 = __builtin_fmaf(a, a, q0); q1 = __builtin_fmaf(b, b, q1); }
    const float q = row_sum(q0 + q1);
    const float sd = rbf(sqrtf(rbf(rbf(q * (1.f / D_)) + 1e-5f)));
    __builtin_amdgcn_raw_buffer_store_b32(__float_as_uint(c15 ? 1.f / sd : -rbf(m)), wsr,
                                          c15 < 2 ? WS_STAT + rb * 2048 + c15 * 1024 + ((row0 & 255) + 4 * p + g) * 4 : OOB, 0, SC1);
    SCHED();
  }
  WAIT_V(0); WAIT_L0(); BAR();
  if (threadIdx.x == 0) flag_st(ws, WS_FP + (c * 8 + rb) * 8, tag);
  STAMP(2);
  wtouch(WROW0, (640 - WROW0 + 95) / 96);
  if (wave < 4) {
    const u32 fso = WS_FP + (wave * 64 + lane) * 8;
    flag_wait<SPSLEEP>(ws, fso, tag, 0);
  }
  BAR();
  STAMP(13);
  if (c == 31 && rb == 0 && threadIdx.x == 0) flag_st(ws, WS_EPOCH, tag);
  if (wave < 4) {
    const u32x4 srdp = mk_srd(ws, WS_END);
    const u32 lds0 = LDS0, l16 = lane * 16;
    const u32 psrc = WS_WDF + (cb2 * 54 + d_k0(wave) + d_step(4, d_rot())) * 5120;
    asm volatile("s_nop 4" ::: "memory");
#pragma unroll
    for (int i = 0; i < 20; i++) DMA16C(lds0 + p_off(i) + p_base(wave, i), l16, srdp, psrc + i * 1024);
  }
  STAMP(4);
}

DI void phase2(LAS char* smem, const u16* x, u16* out, int rb, int cb, int red, char* ws, u64 tag SP_ARG) {
  const int lane = threadIdx.x & 63;
  const int wave = __builtin_amdgcn_readfirstlane(threadIdx.x >> 6);
  const int w = wave & 3;
  const u32 l16 = lane * 16;
  const u32 lds0 = LDS0;
  const rsrc_t wsr = __builtin_amdgcn_make_buffer_rsrc(ws, 0, WS_END, 0x00020000);
  const f32x4 z4 = {0, 0, 0, 0};
  auto tile = [&](int i) { return (w + 1 + i) & 3; };
  u32x2 xr[3];
  u32 xo;
  auto ldx = [&](int n0, int n1) {
    u32 l = l16;
    asm volatile("" : "+v"(l));
    xo = (rb * 64 + w * 16 + ((l >> 4) & 15)) * D_ + cb * 80 + (l >> 8) * 4;
#pragma unroll
    for (int n = n0; n < n1; n++) xr[n - n0] = *(const u32x2*)(x + xo + n * 16);
  };
  auto dpart = [&](int k, int n) { const int f = (3 - k) * 5 + n; return *(LAS f32x4*)(smem + p_off(f) + p_base((w + k) & 3, f) + l16); };
  auto spart = [&](int k, int n) { return *(LAS f32x4*)(smem + L_R + ((w + k) & 3) * R_SZ + ((3 - k) * 5 + n - (k == 0 ? 3 : 0)) * 1024 + l16); };
  auto finish = [&](f32x4 bs, f32x4 ss, u32x2 xv, int n) {
    float r[4];
#pragma unroll
    for (int j = 0; j < 4; j++) {
      const float v = bs[j], gt = v * __builtin_amdgcn_rcpf(1.f + __builtin_amdgcn_exp2f(-1.44269504089f * v));
      r[j] = ((j & 1) ? bhi(xv[j >> 1]) : blo(xv[j >> 1])) + ss[j] * gt;
    }
    *(u32x2*)(out + xo + n * 16) = u32x2{pk2(r[0], r[1]), pk2(r[2], r[3])};
  };
  if (wave < 4) {
    const int k0 = d_k0(w);
    const int ha = WS_HF + (rb * 54 + k0) * 4096, wa = WS_WDF + (cb * 54 + k0) * 5120;
    const int rot = d_rot();
    auto vof = [&](int s) { return (s == 3 || s == 8 || s == 13) ? l16 + ((w >= 2 && s + rot == 13) ? OOB : 0) : l16; };
    u32x4 a[14][4], b[14][5];
    auto lda = [&](int s) {
#pragma unroll
      for (int t = 0; t < 4; t++) a[s][t] = LD128(wsr, vof(s), ha + d_step(s, rot) * 4096 + tile(t) * 1024, LDAUX);
    };
    auto ldb = [&](int s) {
#pragma unroll
      for (int n = 0; n < 5; n++) b[s][n] = LD128(wsr, vof(s) + (n & 3) * 1024, wa + d_step(s, rot) * 5120 + (n >> 2) * 4096, LWAUX);
    };
    auto ldl = [&](int s) {
#pragma unroll
      for (int n = 0; n < 5; n++) { const int f = (s - 4) * 5 + n; b[s][n] = *(LAS u32x4*)(smem + p_off(f) + p_base(w, f) + l16); }
    };
    __builtin_amdgcn_s_setprio(3);
#pragma unroll
    for (int s = 0; s < 4; s++) ldb(s);
    SCHED();
    STAMP(7);
    __builtin_amdgcn_s_setprio(DP_POLL);
    if (red >= 0) {
      const int hg = red >> 1, q = (red & 1) * 216 + threadIdx.x;
      group_wait<8, 2>(smem, L_CNT + 32, w == 0, ws, WS_FKV + hg * 128, tag);
      STAMP(3);
      f32x4 kp[8];
#pragma unroll
      for (int p = 0; p < 8; p++)
        kp[p] = __builtin_bit_cast(f32x4, LD128(wsr, threadIdx.x < 216 && q < 420 ? q * 16 : OOB, WS_KVP + (hg * 8 + p) * 6720, SC1));
      const f32x4 t = ((kp[0] + kp[1]) + (kp[2] + kp[3])) + ((kp[4] + kp[5]) + (kp[6] + kp[7]));
      ST64((u32x2{pk2(t[0], t[1]), pk2(t[2], t[3])}), wsr, threadIdx.x < 216 ? q * 8 : OOB, WS_KVC + hg * 3456);
      arrive(smem, L_CNT + 8, 4, ws, WS_FK + red * 8, tag, 32, 128);
      lds_wait(smem, L_CNT + 8, 4);
      STAMP(5);
    }
    group_wait<18, DSLEEP>(smem, L_CNT + 36, w == 0 && red < 0, ws, WS_FG + rb * 256, tag);
    STAMP(8);
    __builtin_amdgcn_s_setprio(3);
    SCHED();
#pragma unroll
    for (int s = 0; s < 5; s++) lda(s);
    SCHED();
    __builtin_amdgcn_s_setprio(DP_RUN);
    f32x4 acc[4][5];
#pragma unroll
    for (int s = 0; s < 14; s++) {
      SCHED();
#pragma unroll
      for (int t = 0; t < 4; t++)
#pragma unroll
        for (int n = 0; n < 5; n++) {
          acc[t][n] = MFMA16(b[s][n], a[s][t], (s == 0 ? z4 : acc[t][n]));
        }
      if (s == 1) { lda(5); lda(6); }
      if (s == 2) { lda(7); }
      if (s == 3) { lda(8); }
      if (s == 4) { ldb(8); lda(9); }
      if (s == 5) { ldb(9); }
      if (s == 6) { lda(10); }
      if (s == 7) { ldb(10); }
      if (s == 8) { lda(11); ldb(11); }
      if (s == 9) { lda(12); ldb(12); }
      if (s == 10) { lda(13); ldb(13); }
      if (s == 2) ldl(4);
      if (s >= 4 && s <= 6) ldl(s + 1);
      if (s == 11) ldx(3, 5);
#pragma unroll
      for (int i = 0; i < 10; i++) {
        __builtin_amdgcn_sched_group_barrier(0x8, 2, 2);
        __builtin_amdgcn_sched_group_barrier(0x20, 1, 2);
        __builtin_amdgcn_sched_group_barrier(0x100, 1, 2);
      }
      SCHED();
      if (s == 0) STAMP(9);
    }
    STAMP(10);
#pragma unroll
    for (int f = 0; f < 18; f++) *(LAS f32x4*)(smem + p_off(f) + p_base(w, f) + l16) = acc[f / 5][f % 5];
    WAIT_L0(); BAR();
    STAMP(11);
#pragma unroll
    for (int n = 3; n < 5; n++) {
      const f32x4 bs = (acc[3][n] + dpart(1, n)) + (dpart(2, n) + dpart(3, n));
      const f32x4 ss = (spart(0, n) + spart(1, n)) + (spart(2, n) + spart(3, n));
      finish(bs, ss, xr[n - 3], n);
    }
  } else {
    const int g = lane >> 4, c15 = lane & 15;
    const u32x4 srdp = mk_srd(ws, WS_END);
    const u32 rl = lds0 + L_R + w * R_SZ;
    __builtin_amdgcn_s_setprio(SP_W);
    u32x4 wf[5][5], pq[4][2][2];
    u32x4 ph[4];
    const int wa = WS_WOF + (cb * 20 + w * 5) * 5120;
#pragma unroll
    for (int s = 0; s < 3; s++)
#pragma unroll
      for (int n = 0; n < 5; n++) wf[s][n] = LD128(wsr, l16 + (n & 3) * 1024, wa + s * 5120 + (n >> 2) * 4096, SC1);
    asm volatile("s_nop 4" ::: "memory");
    if (red >= 0)
    {
#pragma unroll
      for (int i = 0; i < 10; i++) DMA16C(rl + i * 1024, l16, srdp, wa + 3 * 5120 + i * 1024);
    }
    SCHED();
    STAMP(1);
    constexpr int ga[14][2] = {{0, 0}, {0, 1}, {1, 1}, {1, 2}, {2, 2}, {2, 3}, {3, 3}, {3, 4}, {4, 0}, {4, 1}, {4, 2}, {5, 2}, {5, 3}, {5, 4}};
    u32 gad[14];
#pragma unroll
    for (int k = 0; k < 14; k++) {
      const int j = ga[k][0], hd = j < 4 ? j : 2 * (j - 4) + (c15 & 1), row = j < 4 ? c15 : (c15 & 2) ? 20 : 16 + (c15 >> 2);
      const u32 d = 16 * ga[k][1] + 4 * g - 20 * hd;
      gad[k] = rl + 10240 + (d < 20u ? hd * 840 + row * 40 + d * 2 : 3360);
      asm volatile("" : "+v"(gad[k]));
    }
    STAMP(6);
    group_wait<4>(smem, L_CNT + 44, w == 0, ws, WS_FG + rb * 256 + 208, tag);
    STAMP(7);
#pragma unroll
    for (int h = 0; h < 2; h++)
#pragma unroll
      for (int t = 0; t < 4; t++) {
        const int qa = WS_QF + ((rb * 4 + tile(t)) * 8 + 2 * w + h) * 2560;
        pq[t][h][0] = LD128(wsr, l16, qa, LDAUX); pq[t][h][1] = LD128(wsr, l16 + 1024, qa, LDAUX);
        const u32x2 v = LD64(wsr, lane * 8 + 2048, qa, LDAUX);
        ph[t][2 * h] = v[0]; ph[t][2 * h + 1] = v[1];
      }
    SCHED();
    __builtin_amdgcn_s_setprio(SP_POLL);
    if (red >= 0 && w == 0) group_wait<18>(smem, L_CNT + 36, true, ws, WS_FG + rb * 256, tag);
    group_wait<16>(smem, L_CNT + 40, w == 0, ws, WS_FK + rb * 128, tag);
    __builtin_amdgcn_s_setprio(SP_HI);
    STAMP(8);
#pragma unroll
    for (int i = 0; i < 7; i++) DMA16C(rl + 10240 + i * 1024, l16, srdp, WS_KVC + w * 6912 + i * 1024);
    WAIT_V(0);
    STAMP(9);
    SCHED();
    {
      u32x2 gq[2][14];
      f32x4 on[2][6];
      const f32x4 e4 = {0.f, 0.f, 1e-6f, 1e-6f};
      auto mm = [&](int u) {
        const int h = u >> 2, t = u & 3;
        auto fr2 = [&](int lo, int hi) { return u32x4{lo < 0 ? 0u : gq[h][lo][0], lo < 0 ? 0u : gq[h][lo][1], hi < 0 ? 0u : gq[h][hi][0], hi < 0 ? 0u : gq[h][hi][1]}; };
        auto fr5 = [&](int k) { return h ? fr2(-1, k) : fr2(k, -1); };
        f32x4(&o)[6] = on[u & 1];
        const u32x4 p0 = pq[t][h][0], p1 = pq[t][h][1], p2 = ph[t];
        o[4] = MFMA16(fr2(8, 9), p0, e4);
        o[5] = MFMA16(fr2(11, 12), p1, e4);
        o[1] = MFMA16(fr2(-1, 2), p0, z4);
        o[3] = MFMA16(fr2(-1, 6), p1, z4);
        o[4] = MFMA16(fr2(10, -1), p1, o[4]);
        o[5] = MFMA16(fr5(13), p2, o[5]);
        o[0] = MFMA16(fr2(0, 1), p0, z4);
        o[2] = MFMA16(fr2(4, 5), p1, z4);
        o[1] = MFMA16(fr2(3, -1), p1, o[1]);
        o[3] = MFMA16(fr5(7), p2, o[3]);
      };
      auto scale = [&](int u) {
        const int h = u >> 2, t = u & 3;
        f32x4(&o)[6] = on[u & 1];
        float rd[4];
#pragma unroll
        for (int j = 0; j < 4; j++) rd[j] = __builtin_amdgcn_rcpf(o[4 + (j >> 1)][2 + (j & 1)]);
        u32 op[4][2];
#pragma unroll
        for (int j = 0; j < 4; j++) { op[j][0] = pk2(o[j][0] * rd[j], o[j][1] * rd[j]); op[j][1] = pk2(o[j][2] * rd[j], o[j][3] * rd[j]); }
        pq[t][h][0] = u32x4{op[0][0], op[0][1], op[1][0], op[1][1]};
        pq[t][h][1] = u32x4{op[2][0], op[2][1], op[3][0], op[3][1]};
        ph[t][2 * h] = pk2(o[4][0] * rd[0], o[4][1] * rd[1]); ph[t][2 * h + 1] = pk2(o[5][0] * rd[2], o[5][1] * rd[3]);
      };
      auto gather = [&](int h) {
#pragma unroll
        for (int k = 0; k < 14; k++) gq[h][k] = *(LAS u32x2*)((LAS char*)(size_t)gad[k] + h * 3456);
      };
      gather(0);
#pragma unroll
      for (int u = 0; u < 9; u++) {
        if (u == GPOS) gather(1);
        if (u == 4 && GPOS == 4) { scale(3); SCHED(); mm(4); }
        else {
          if (u < 8) mm(u);
          if (u > 0) scale(u - 1);
#pragma unroll
          for (int i = 0; i < 10; i++) { __builtin_amdgcn_sched_group_barrier(0x8, 1, 1); __builtin_amdgcn_sched_group_barrier(0x2, 3, 1); }
        }
        SCHED();
        if (u == 0) STAMP(13);
        if (u == 3) STAMP(14);
        if (u == 4) STAMP(5);
      }
    }
    STAMP(10);
#pragma unroll
    for (int i = 0; i < 10; i++) wf[3 + i / 5][i % 5] = *(LAS u32x4*)((LAS char*)(size_t)rl + i * 1024 + l16);
    ldx(0, 3);
    WAIT_L0();
    f32x4 acc[5];
#pragma unroll
    for (int t = 0; t < 4; t++) {
#pragma unroll
      for (int s = 0; s < 5; s++) {
        const u32x4 o = s < 4 ? pq[t][s >> 1][s & 1] : ph[t];
#pragma unroll
        for (int n = 0; n < 5; n++) acc[n] = MFMA16(wf[s][n], o, (s == 0 ? z4 : acc[n]));
      }
#pragma unroll
      for (int n = (t == 3 ? 3 : 0); n < 5; n++) *(LAS f32x4*)((LAS char*)(size_t)rl + (t * 5 + n - (t == 3 ? 3 : 0)) * 1024 + l16) = acc[n];
    }
    STAMP(12);
    WAIT_L0(); BAR();
    STAMP(11);
#pragma unroll
    for (int n = 0; n < 3; n++) {
      const f32x4 bs = (dpart(0, n) + dpart(1, n)) + (dpart(2, n) + dpart(3, n));
      const f32x4 ss = (acc[n] + spart(1, n)) + (spart(2, n) + spart(3, n));
      finish(bs, ss, xr[n], n);
    }
  }
}

__global__ __launch_bounds__(512, 2) void fused(const u16* __restrict__ x, const u16* __restrict__ wqkv, const u16* __restrict__ wo,
                                                const u16* __restrict__ wgt, const u16* __restrict__ wu, const u16* __restrict__ wd,
                                                u16* __restrict__ out, char* ws) {
  extern __shared__ __attribute__((aligned(16))) char smem_raw[];
  LAS char* smem = (LAS char*)smem_raw;
  STAMP_INIT(); STAMP(0);
  const int xcd = blockIdx.x & 7, idx = blockIdx.x >> 3;
  const int rb = (xcd >> 2) * 4 + (idx < 28 ? (idx & 3) : (xcd & 2) + (idx & 1));
  const int c = idx < 28 ? (idx >> 2) * 4 + (xcd & 3) : 28 + (idx & 2) + (xcd & 1);
  const int rb2 = xcd * 4 + (idx >> 3), cb2 = idx & 7;
  u64 tag;
  if (threadIdx.x < 16) ((LAS u32*)(smem + L_CNT))[threadIdx.x] = 0;
  u64 pc;
  asm volatile("s_getpc_b64 %0" : "=s"(pc));
  if (__builtin_amdgcn_readfirstlane(threadIdx.x >> 6) == 7) {
    const u32x4 srdc = mk_srd((const void*)pc, CODEWARM);
    asm volatile("s_nop 4" ::: "memory");
    TOUCH(LDS0 + L_DUMMY, (u32)((idx & 7) * 8192 + (threadIdx.x & 63) * 128), srdc, 0);
  }
  if (c >= 30) spare(smem, x, wqkv, wgt, wu, rb, c, cb2, ws, tag SP_PASS);
  else {
    const int lane = threadIdx.x & 63;
    const int wave = __builtin_amdgcn_readfirstlane(threadIdx.x >> 6);
    const int grp = wave >> 2, wg = wave & 3, img = wg >> 1;
    const u32 lds0 = LDS0;
    P1 p;
    const u32x4 srdx = mk_srd(x + (size_t)rb * 256 * D_, 256 * D_ * 2);
    u32 vx[2];
#pragma unroll
    for (int i = 0; i < 2; i++) { const int r = 8 * i + (lane >> 3); vx[i] = (64 * wg + r) * (D_ * 2) + (((lane & 7) ^ ((r >> 1) & 7)) << 4); }

    const int unit = ((c < 18 ? c : c >= 26 ? c - 8 : c + 4) * 8 + rb) * 4 + wg,
              uu = unit < 740 ? unit : 739, uks = uu / 10, ucg = uu - uks * 10;
    asm volatile("s_nop 4" ::: "memory");
#pragma unroll
    for (int i = 0; i < 8; i++) DMA16C(lds0 + grp * L_X1 + wg * 8192 + i * 1024, vx[i & 1], srdx, grp * 128 + (i >> 1) * 16 * D_ * 2);
    int col;
    if (c < 18) col = c * 96;
    else if (c < 26) col = 640 + img * 640 + 80 * (c - 18) - 16 * (c & 1);
    else col = 160 * (c - 26) + img * 64;
    p.pitch = c < 18 ? F_ * 2 : 3840;
    const u16* wb = (c < 18 ? (img ? wu : wgt) : wqkv) + col;
    const u32x4 srdw = mk_srd(wb, D_ * p.pitch - col * 2);
#pragma unroll
    for (int i = 0; i < 6; i++) { const int o = (6 * (wg & 1) + i) * 1024 + lane * 16, r = o / 192; p.vo[i] = r * p.pitch + ((o - r * 192) ^ ((r & 8) << 2)); }
    asm volatile("s_nop 4" ::: "memory");
#pragma unroll
    for (int i = 0; i < 6; i++) DMA16W(lds0 + (grp ? L_W1 : L_W0) + wg * 6144 + i * 1024, p.vo[i], srdw, grp * 64 * p.pitch);
    if (grp) {
      int r0, r1;
      bool rem = false;
      if (uks < 54) { r0 = 32 * uks; r1 = r0 + 16; }
      else {
        const int k = uks - 54, pr = k / 5, s5 = k - pr * 5;
        if (s5 < 4) { r0 = 160 * pr + 40 * s5; r1 = r0 + 20; } else { r0 = 160 * pr + 16; r1 = r0 + 80; rem = true; }
      }
      const u32x4 srdu = uks < 54 ? mk_srd(wd, AB_U * F_ * D_ * 2) : mk_srd(wo, unit < 740 ? AB_U * D_ * D_ * 2 : 0);
      const u32 vu = (rem ? 20 * ((lane >> 3) & 3) + (lane >> 5) : lane >> 3) * (D_ * 2) + (lane & 7) * 16;
      asm volatile("s_nop 4" ::: "memory");
#pragma unroll
      for (int i = 0; i < 4; i++) DMA16(lds0 + L_SP + wg * 4096 + i * 1024, vu, srdu, ((i < 2 ? r0 : r1) + (rem ? 2 : 8) * (i & 1)) * (D_ * 2) + ucg * 128);
    }
    {
      const int g = lane >> 4;
      const bool alt = uks < 54 && uks % 3 == 1;
      p.ua = lds0 + L_SP + wg * 4096 + ((alt ? 16 * (g >> 1) + 4 * (g & 1) : 4 * g) + ((lane & 15) >> 2)) * 128 + (lane & 3) * 8;
      p.ub = p.ua + (alt ? 8 * 128 : 16 * 128);
#pragma unroll
      for (int j = 0; j < 4; j++) {
        const int n16 = ucg * 4 + j, cbk = n16 / 5, nt = n16 - cbk * 5;
        p.ud[j] = unit >= 740 ? OOB : (uks < 54 ? WS_WDF + (cbk * 54 + uks) * 5120 : WS_WOF + (cbk * 20 + uks - 54) * 5120) + nt * 1024;
      }
    }
    tag = get_tag(ws);
    if (grp == 0) {
      p.srd = srdx; p.vo[0] = vx[0]; p.vo[1] = vx[1];
    } else {
      p.srd = srdw;
    }
    STAMP(12);
    if (c < 18) phase1<0>(smem, p, 0, 0, rb, c, c, cb2, ws, tag SP_PASS);
    else if (c < 26) phase1<1>(smem, p, c & 1, c & 1, rb, c - 18, c, cb2, ws, tag SP_PASS);
    else phase1<2>(smem, p, 0, 1, rb, c - 26, c, cb2, ws, tag SP_PASS);
  }
  phase2(smem, x, out, rb2, cb2, c >= 30 ? rb * 2 + c - 30 : -1, ws, tag SP_PASS);
  STAMP(15); STAMP_FLUSH(ws);
}

extern "C" void kernel_launch(void* const* d_in, const int* in_sizes, int n_in, void* d_out, int out_size, void* d_ws, size_t ws_size,
                              hipStream_t stream) {
  (void)hipFuncSetAttribute((const void*)fused, hipFuncAttributeMaxDynamicSharedMemorySize, LDS_BYTES);
  fused<<<256, 512, LDS_BYTES, stream>>>((const u16*)d_in[0], (const u16*)d_in[1], (const u16*)d_in[2], (const u16*)d_in[4],
                                         (const u16*)d_in[5], (const u16*)d_in[6], (u16*)d_out, (char*)d_ws);
}
```

```cpp
#include <hip/hip_runtime.h>
#include <type_traits>
typedef unsigned short u16;
typedef unsigned int u32;
typedef unsigned long long u64;
typedef short s16x4 __attribute__((ext_vector_type(4)));
typedef short s16x8 __attribute__((ext_vector_type(8)));
typedef __bf16 bf16x8 __attribute__((ext_vector_type(8)));
typedef __bf16 bf16x2 __attribute__((ext_vector_type(2)));
typedef float f32x2 __attribute__((ext_vector_type(2)));
typedef float f32x4 __attribute__((ext_vector_type(4)));
typedef u32 u32x2 __attribute__((ext_vector_type(2)));
typedef u32 u32x4 __attribute__((ext_vector_type(4)));
#define LAS __attribute__((address_space(3)))
#define GAS __attribute__((address_space(1)))
#define DI __device__ __forceinline__
#define LDS0 ((u32)__builtin_amdgcn_readfirstlane((u32)(size_t)smem))

constexpr int S_ = 2048, D_ = 640, F_ = 1728;
constexpr int WS_EPOCH = 0;
constexpr int WS_FG   = 4096;
constexpr int WS_FKV  = WS_FG + 8192;
constexpr int WS_FP   = WS_FKV + 1024;
constexpr int WS_FK   = 24576;
constexpr int WS_STAT = 507904;
constexpr int WS_KVC  = 32768;
constexpr int WS_KVP  = 65536;
constexpr int WS_HF   = 1 << 20;
constexpr int WS_QF   = WS_HF + 32 * 54 * 4096;
constexpr int WS_WDF  = WS_QF + 128 * 20 * 1024;
constexpr int WS_WOF  = WS_WDF + 8 * 54 * 5120;
constexpr int WS_END  = WS_WOF + 8 * 20 * 5120;
constexpr u64 TAG_STEP = 0x9E3779B97F4A7C15ull;
constexpr u32 SPIN_CAP = 1u << 20;
constexpr int SC1 = 16;
constexpr int CODE_BYTES = 51900;
constexpr int OOB = 0x40000000;

constexpr int L_X0 = 0, L_W1 = 32768, L_W2 = 57344, L_X1 = 81920, L_W0 = 114688, L_SP = 139264;
constexpr int W_IMG1 = 12288;
constexpr int L_R = 81920, R_SZ = 19968;
constexpr int L_ST = 158976;
constexpr int L_DUMMY = 161792;
constexpr int L_CS = L_R + 17408;
constexpr int L_CNT = 163584;
constexpr int LDS_BYTES = 163840;
DI constexpr int p_off(int f) { return f < 8 ? L_X0 + f * 1024 : f < 14 ? L_W1 + (f - 8) * 1024 : L_W2 + (f - 14) * 1024; }
DI int p_base(int w, int f) { return w * (f < 8 ? 8192 : 6144); }

DI u32 pk2(float a, float b) { f32x2 v = {a, b}; return __builtin_bit_cast(u32, __builtin_convertvector(v, bf16x2)); }
DI u32 pkm(float a, float b, float c, float d) { return pk2(a * b, __builtin_fmaf(c, d, 0.f)); }
DI float blo(u32 d) { return __uint_as_float(d << 16); }
DI float bhi(u32 d) { return __uint_as_float(d & 0xffff0000u); }
DI float rbf(float f) { return blo(pk2(f, f)); }
DI u32x4 mk_srd(const void* p, u32 bytes) {
  u64 a = (u64)p;
  u32x4 r;
  r[0] = __builtin_amdgcn_readfirstlane((u32)a); r[1] = __builtin_amdgcn_readfirstlane((u32)(a >> 32) & 0xffffu);
  r[2] = __builtin_amdgcn_readfirstlane(bytes); r[3] = 0x00020000u;
  return r;
}
#define DMA16NT(m0v, voff, srd, soff) \
  asm volatile("s_mov_b32 m0, %0\n\ts_nop 0\n\tbuffer_load_dwordx4 %1, %2, %3 offen nt lds" ::"s"(m0v), "v"(voff), "s"(srd), "s"(soff) : "memory")
#define DMA16C(m0v, voff, srd, soff) \
  asm volatile("s_mov_b32 m0, %0\n\ts_nop 0\n\tbuffer_load_dwordx4 %1, %2, %3 offen sc1 lds" ::"s"(m0v), "v"(voff), "s"(srd), "s"(soff) : "memory")
#define TOUCH(m0v, voff, srd, soff) \
  asm volatile("s_mov_b32 m0, %0\n\ts_nop 0\n\tbuffer_load_dword %1, %2, %3 offen sc1 lds" ::"s"(m0v), "v"(voff), "s"(srd), "s"(soff) : "memory")
#define WAIT_V(n) asm volatile("s_waitcnt vmcnt(%0)" :: "i"(n) : "memory")
#define WAIT_L0() do { asm volatile("" ::: "memory"); __builtin_amdgcn_s_waitcnt(0xC07F); asm volatile("" ::: "memory"); } while (0)
#define BAR() do { asm volatile("" ::: "memory"); __builtin_amdgcn_s_barrier(); asm volatile("" ::: "memory"); } while (0)
#define SCHED() __builtin_amdgcn_sched_barrier(0)
#define MFMA16(a, b, c) __builtin_amdgcn_mfma_f32_16x16x32_bf16(__builtin_bit_cast(bf16x8, a), __builtin_bit_cast(bf16x8, b), c, 0, 0, 0)
#define LD128(r, v, s, aux) __builtin_amdgcn_raw_buffer_load_b128(r, v, s, aux)
#define LD64(r, v, s, aux) __builtin_amdgcn_raw_buffer_load_b64(r, v, s, aux)
#define ST128(x, r, v, s) __builtin_amdgcn_raw_buffer_store_b128(x, r, (v) + (s), 0, SC1)
#define ST64(x, r, v, s) __builtin_amdgcn_raw_buffer_store_b64(x, r, (v) + (s), 0, SC1)
typedef __amdgpu_buffer_rsrc_t rsrc_t;

DI u64 flag_ld(char* ws, u32 off) { return __hip_atomic_load((GAS u64*)(ws + off), __ATOMIC_RELAXED, __HIP_MEMORY_SCOPE_AGENT); }
DI void flag_st(char* ws, u32 off, u64 v) { __hip_atomic_store((GAS u64*)(ws + off), v, __ATOMIC_RELAXED, __HIP_MEMORY_SCOPE_AGENT); }
template <int SLEEP = 2>
DI void flag_wait(char* ws, u32 off, u64 tag, u64 v) {
#pragma unroll 1
  for (u32 spins = 0; !__all(v == tag) && spins < SPIN_CAP; spins++) {
    __builtin_amdgcn_s_sleep(SLEEP);
    v = flag_ld(ws, off);
    __builtin_amdgcn_s_waitcnt(0x0F70);
  }
  __builtin_amdgcn_fence(__ATOMIC_ACQUIRE, "wavefront");
}
DI void lds_wait(LAS char* smem, int off, u32 want) {
#pragma unroll 1
  for (u32 spins = 0; spins < (SPIN_CAP << 4); spins++) {
    const u32 v = __hip_atomic_load((LAS u32*)(smem + off), __ATOMIC_RELAXED, __HIP_MEMORY_SCOPE_WORKGROUP);
    if (__builtin_amdgcn_readfirstlane(v) >= want) break;
    __builtin_amdgcn_s_sleep(1);
  }
  asm volatile("" ::: "memory");
}
typedef u64 u64x8 __attribute__((ext_vector_type(8)));
typedef u64 u64x4 __attribute__((ext_vector_type(4)));
typedef u64 u64x2 __attribute__((ext_vector_type(2)));
template <int N>
DI bool flags_set_scalar(char* p, u64 tag) {
  u64 bad = 0;
  if (N == 4) {
    u64x4 a;
    asm volatile("s_load_dwordx8 %0, %1, 0x0 glc\n\ts_waitcnt lgkmcnt(0)" : "=&s"(a) : "s"(p) : "memory");
#pragma unroll
    for (int i = 0; i < 4; i++) bad |= a[i] ^ tag;
  } else if (N == 8) {
    u64x8 a;
    asm volatile("s_load_dwordx16 %0, %1, 0x0 glc\n\ts_waitcnt lgkmcnt(0)" : "=&s"(a) : "s"(p) : "memory");
#pragma unroll
    for (int i = 0; i < 8; i++) bad |= a[i] ^ tag;
  } else {
    u64x8 a, b; u64x2 c;
    if (N == 16) {
      asm volatile("s_load_dwordx16 %0, %2, 0x0 glc\n\ts_load_dwordx16 %1, %2, 0x40 glc\n\ts_waitcnt lgkmcnt(0)" : "=&s"(a), "=&s"(b) : "s"(p) : "memory");
    } else {
      asm volatile("s_load_dwordx16 %0, %3, 0x0 glc\n\ts_load_dwordx16 %1, %3, 0x40 glc\n\ts_load_dwordx4 %2, %3, 0x80 glc\n\ts_waitcnt lgkmcnt(0)"
                   : "=&s"(a), "=&s"(b), "=&s"(c) : "s"(p) : "memory");
      bad = (c[0] ^ tag) | (c[1] ^ tag);
    }
#pragma unroll
    for (int i = 0; i < 8; i++) bad |= (a[i] ^ tag) | (b[i] ^ tag);
  }
  return bad == 0;
}
template <int N, int SLEEP = 6>
DI void group_wait(LAS char* smem, int word, bool leader, char* ws, u32 off, u64 tag) {
  if (leader) {
    const int lane = threadIdx.x & 63;
    const u32 vo = off + (lane < N ? lane : N - 1) * 8;
    bool ok = false;
#pragma unroll 1
    for (int i = 0; i < 64 && !(ok = flags_set_scalar<N>(ws + off, tag)); i++) __builtin_amdgcn_s_sleep(5);
    if (!ok) flag_wait<SLEEP>(ws, vo, tag, flag_ld(ws, vo));
    *(volatile LAS u32*)(smem + word) = 1;
  } else lds_wait(smem, word, 1);
}
DI void arrive(LAS char* smem, int cnt, u32 n, char* ws, u32 flag, u64 tag, int rep = 1, int stride = 0) {
  WAIT_V(0);
  const int lane = threadIdx.x & 63;
  const u32 k = __hip_atomic_fetch_add((LAS u32*)(smem + (lane ? L_DUMMY + lane * 4 : cnt)), 1u, __ATOMIC_RELAXED, __HIP_MEMORY_SCOPE_WORKGROUP);
  const bool go = __builtin_amdgcn_readfirstlane(k) == n - 1 && lane < rep;
  const rsrc_t wsr = __builtin_amdgcn_make_buffer_rsrc(ws, 0, WS_END, 0x00020000);
  ST64((u32x2{(u32)tag, (u32)(tag >> 32)}), wsr, go ? flag + lane * stride : OOB, 0);
}
DI u64 get_tag(char* ws) {
  u64 e;
  asm volatile("s_load_dwordx2 %0, %1, 0x0\n\ts_waitcnt lgkmcnt(0)" : "=s"(e) : "s"(ws + WS_EPOCH) : "memory");
  return e + TAG_STEP;
}
DI int d_k0(int w) { return w < 2 ? w * 14 : 2 + w * 13; }
DI int d_rot() {
  const int idx = blockIdx.x >> 3;
  return (((idx & 7) + (idx >> 3)) % 3) * 5;
}
DI int d_step(int s, int r) { return s + r >= 14 ? s + r - 14 : s + r; }
struct P1 {
  u32x4 srd;
  u32 vo[6];
  u32 ua, ub, ud[4];
  int pitch;
};

template <int TYPE>
DI void phase1(LAS char* smem, P1& p, int o0, int o1, int rb, int cb, int c, int cb2, char* ws, u64 tag) {
  constexpr int NT = TYPE == 0 ? 6 : 5;
  const int lane = threadIdx.x & 63;
  const int wave = __builtin_amdgcn_readfirstlane(threadIdx.x >> 6);
  const int grp = wave >> 2, wg = wave & 3, wm = wave >> 1, wn = wave & 1;
  const u32 lds0 = LDS0;
  const u32 mb = lds0 + wg * (grp == 0 ? 8192 : 6144);
  const int g = lane >> 4, c15 = lane & 15;
  const u32 l16 = lane * 16;
  constexpr int NB = TYPE == 1 ? 5 : TYPE == 0 ? 3 : 2;
  u32 ax[2], aw[NB];
  const int ksw = wm & 1;
  ax[0] = lds0 + (wm * 64 + c15) * 128 + (((4 * ksw + g) ^ (c15 >> 1)) << 4);
  ax[1] = ax[0] ^ 64;
  {
    const u32 awr = lds0 + (8 * g + (c15 >> 2)) * 192, sw = (g & 1) << 5, p8 = (lane & 3) * 8;
#pragma unroll
    for (int n = 0; n < NB; n++) {
      if (TYPE == 0) aw[n] = awr + (((n < 2 ? 4 * wn + n : 2 + wn) * 32 + p8) ^ sw);
      else if (TYPE == 2) aw[n] = awr + wn * W_IMG1 + ((((wn ? o1 : o0) + n) * 32 + p8) ^ sw);
      else {
        const int j = 32 * n + p8;
        aw[n] = awr + (j < 80 ? 0 : W_IMG1) + ((32 * o0 + 80 * wn + (j < 80 ? j : j - 80)) ^ sw);
      }
    }
  }
  f32x4 acc[4][NT];
#pragma unroll
  for (int m = 0; m < 4; m++)
#pragma unroll
    for (int n = 0; n < NT; n++) acc[m][n] = f32x4{0, 0, 0, 0};
  s16x8 xf[2][4], wf[2][NT];
  const rsrc_t wsr = __builtin_amdgcn_make_buffer_rsrc(ws, 0, WS_END, 0x00020000);
  const u32x4 srdp = mk_srd(ws, WS_END);
  const u32 psrc = WS_WDF + (cb2 * 54 + d_k0(wg) + d_step(4, d_rot())) * 5120;
  u32 sr0 = __builtin_amdgcn_readfirstlane(p.srd[0]), sr1 = __builtin_amdgcn_readfirstlane(p.srd[1]), sr2 = __builtin_amdgcn_readfirstlane(p.srd[2]);
  u32 xstep = 16 * D_ * 2;

  auto trd = [&](LAS char* a) { return __builtin_amdgcn_ds_read_tr16_b64_v4i16((LAS s16x4*)a); };
  auto dma_x = [&](int i, u32 m, u32 so) { const u32x4 srd = {sr0, sr1, sr2, 0x00020000u}; DMA16C(m + i * 1024, p.vo[i & 1], srd, so + (i >> 1) * xstep); };
  auto dma_w = [&](int i, u32 m, u32 so) { const u32x4 srd = {sr0, sr1, sr2, 0x00020000u}; DMA16C(m + i * 1024, p.vo[i], srd, so); };
  u32 bw[NB], bx;
  auto load_q = [&](auto mode, int k2, int q, int d0, int dn, u32 xo, u32 wo, u32 m, u32 so) {
    constexpr int MODE = decltype(mode)::value;
    if (q == 0) {
      const u32 wk = wo + (k2 ^ ksw) * (32 * 192);
      bx = ax[k2] + xo;
#pragma unroll
      for (int n = 0; n < NB; n++) { bw[n] = aw[n] + wk; asm volatile("" : "+v"(bw[n])); }
      asm volatile("" : "+v"(bx));
    }
#pragma unroll
    for (int i = d0 + q * dn; i < d0 + (q + 1) * dn; i++) {
      if (MODE == 1 && i >= 0 && i < 8) dma_x(i, m, so);
      if (MODE == 2 && i >= 0 && i < 6) dma_w(i, m, so);
    }
    if (q == 0) {
#pragma unroll
      for (int mm = 0; mm < 4; mm++) xf[k2][mm] = *(LAS s16x8*)((LAS char*)(size_t)bx + mm * 2048);
    } else {
#pragma unroll
      for (int n = 2 * q - 2; n < 2 * q && n < NT; n++) {
        LAS char* a = (LAS char*)(size_t)bw[n % NB] + (TYPE == 0 ? (n / 3) * W_IMG1 : TYPE == 2 ? (n / 2) * 64 : 0);
        s16x4 lo = trd(a), hi = trd(a + 4 * 192);
        wf[k2][n] = __builtin_shufflevector(lo, hi, 0, 1, 2, 3, 4, 5, 6, 7);
      }
    }
  };
  auto load_half = [&](auto mode, int k2, int d0, int dn, u32 xo, u32 wo, u32 m, u32 so) {
#pragma unroll
    for (int q = 0; q < 4; q++) load_q(mode, k2, q, d0, dn, xo, wo, m, so);
  };
  auto load_phase = [&](auto mode, u32 xo, u32 wo, u32 m, u32 so) {
    constexpr int MODE = decltype(mode)::value;
    load_half(mode, 0, MODE == 1 ? 0 : -1, 1, xo, wo, m, so);
    load_half(mode, 1, MODE == 1 ? 4 : 3, 1, xo, wo, m, so);
  };
  auto comp_q = [&](int k2, int m) {
#pragma unroll
    for (int n = 0; n < NT; n++)
      acc[m][n] = TYPE == 1 ? MFMA16(xf[k2][m], wf[k2][n], acc[m][n]) : MFMA16(wf[k2][n], xf[k2][m], acc[m][n]);
  };
  auto comp_half = [&](int k2) {
#pragma unroll
    for (int m = 0; m < 4; m++) comp_q(k2, m);
  };
  auto compute = [&]() { comp_half(0); comp_half(1); };
  f32x4 cm[NT];
#pragma unroll
  for (int n = 0; n < NT; n++) cm[n] = f32x4{0, 0, 0, 0};
  const u32x4 ones = {0x3f803f80u, 0x3f803f80u, 0x3f803f80u, 0x3f803f80u};
  auto colsum = [&]() {
#pragma unroll
    for (int n = 0; n < NT; n++) cm[n] = MFMA16(ones, wf[0][n], cm[n]);
  };
  using I0 = std::integral_constant<int, 0>; using I1 = std::integral_constant<int, 1>; using I2 = std::integral_constant<int, 2>;
  asm volatile("" : "+v"(ax[0]), "+v"(ax[1]));
#pragma unroll
  for (int n = 0; n < NB; n++) asm volatile("" : "+v"(aw[n]));
  if (grp == 0) {
    WAIT_V(0);
    if (lane == 0) __hip_atomic_fetch_add((LAS u32*)(smem + L_CNT + 60), 1u, __ATOMIC_RELAXED, __HIP_MEMORY_SCOPE_WORKGROUP);
    lds_wait(smem, L_CNT + 60, 4);
    SCHED();
    load_phase(I0{}, L_X0, L_W0, 0, 0); SCHED(); colsum(); compute(); SCHED();
    BAR(); SCHED();
    u32 w_cur = L_W1, w_nxt = L_W2, w_prv = L_W0, so = 2 * 128, xo = L_X1;
    int left = 8;
#pragma unroll 1
    for (int seg = 0;; seg++) {
#pragma unroll 1
      for (;;) {
        load_phase(I1{}, xo, w_cur, mb + (L_X1 - xo), so);
        { const u32 u = w_prv; w_prv = w_cur; w_cur = w_nxt; w_nxt = u; }
        so += 128; xo = L_X1 - xo;
        SCHED(); colsum(); compute(); SCHED();
        if (--left == 0) break;
        WAIT_V(0); BAR(); SCHED();
      }
      if (seg) break;
      sr0 = srdp[0]; sr1 = srdp[1]; sr2 = srdp[2]; so = psrc; xstep = 2048; p.vo[0] = l16; p.vo[1] = l16 + 1024; left = 1;
      WAIT_V(0); BAR(); SCHED();
    }
    BAR(); SCHED();
    if (lane < 16) {
#pragma unroll
      for (int n = 0; n < NT; n++) ((LAS float*)(smem + L_CS))[((ksw * 2 + wn) * 6 + n) * 16 + lane] = cm[n][0];
    }
    asm volatile("" ::: "memory");
    if (lane == 0) __hip_atomic_fetch_add((LAS u32*)(smem + L_CNT + 4), 1u, __ATOMIC_RELAXED, __HIP_MEMORY_SCOPE_WORKGROUP);
#pragma unroll
    for (int i = 0; i < 10; i++) DMA16C(lds0 + L_R + wg * R_SZ + i * 1024, l16, srdp, WS_WOF + (cb2 * 20 + wg * 5 + 3) * 5120 + i * 1024);
  } else {
    __builtin_amdgcn_s_setprio(1);
#pragma unroll
    for (int m = 0; m < 4; m++)
#pragma unroll
      for (int n = 0; n < NT; n++) asm volatile("" : "+v"(acc[m][n]));
    lds_wait(smem, L_CNT + 60, 4);
    SCHED();
    u32 w_cur = L_W0, w_nxt = L_W1, w_prv = L_W2, so = 2 * 64 * p.pitch, sstep = 64 * p.pitch, xo = L_X0;
    {
      u32 uq[6] = {(u32)OOB, (u32)OOB, p.ud[0], p.ud[1], p.ud[2], p.ud[3]};
      p.ua -= 64; p.ub -= 64;
#pragma unroll 1
      for (int t = 0; t < 3; t++) {
        load_phase(I2{}, xo, w_cur, mb + w_prv, so);
        { const u32 u = w_prv; w_prv = w_cur; w_cur = w_nxt; w_nxt = u; }
        so += sstep; xo = L_X1 - xo;
#pragma unroll
        for (int j = 0; j < 2; j++) {
          const s16x4 lo = trd((LAS char*)(size_t)p.ua + j * 32), hi = trd((LAS char*)(size_t)p.ub + j * 32);
          ST128(__builtin_bit_cast(u32x4, __builtin_shufflevector(lo, hi, 0, 1, 2, 3, 4, 5, 6, 7)), wsr, l16, uq[j]);
        }
        p.ua += 64; p.ub += 64;
#pragma unroll
        for (int j = 0; j < 4; j++) uq[j] = uq[j + 2];
        WAIT_L0(); WAIT_V(8);
        SCHED(); BAR(); SCHED();
        compute(); SCHED();
      }
    }
    arrive(smem, L_CNT + 12, 4, ws, WS_FP + (c * 8 + rb) * 8, tag);
    int left = 4;
#pragma unroll 1
    for (int seg = 1;; seg++) {
#pragma unroll 1
      do {
        load_phase(I2{}, xo, w_cur, mb + w_prv, so);
        { const u32 u = w_prv; w_prv = w_cur; w_cur = w_nxt; w_nxt = u; }
        so += sstep; xo = L_X1 - xo;
        WAIT_L0(); WAIT_V(6);
        SCHED(); BAR(); SCHED();
        compute(); SCHED();
      } while (--left);
      if (seg == 3) break;
      left = seg;
      if (seg == 1) {
        DMA16C(lds0 + L_SP + wg * 4096, l16, srdp, WS_FP); DMA16C(lds0 + L_SP + wg * 4096 + 1024, l16, srdp, WS_FP + 1024);
      }
      if (seg == 2) {
        const u32x4 fa = *(LAS u32x4*)(smem + L_SP + wg * 4096 + l16), fb = *(LAS u32x4*)(smem + L_SP + wg * 4096 + 1024 + l16);
        const u32 t0 = (u32)tag, t1 = (u32)(tag >> 32);
        const u32 bad = ((fa[0] ^ t0) | (fa[1] ^ t1) | (fa[2] ^ t0) | (fa[3] ^ t1)) | ((fb[0] ^ t0) | (fb[1] ^ t1) | (fb[2] ^ t0) | (fb[3] ^ t1));
        if (__builtin_amdgcn_ballot_w64(bad != 0) != 0) {
#pragma unroll 1
          for (int i = 0; i < 4; i++) flag_wait<4>(ws, WS_FP + i * 512 + lane * 8, tag, flag_ld(ws, WS_FP + i * 512 + lane * 8));
          __builtin_amdgcn_s_waitcnt(0x0F70);
        }
        sr0 = srdp[0]; sr1 = srdp[1]; sr2 = srdp[2]; so = psrc + 8192; sstep = 6144;
        DMA16C(lds0 + L_ST + (wg & 1) * 1024, l16, srdp, WS_STAT + rb * 2048 + (wg & 1) * 1024);
#pragma unroll
        for (int i = 0; i < 6; i++) p.vo[i] = l16 + i * 1024;
      }
      SCHED();
    }
    __builtin_amdgcn_s_setprio(0);
  }
  LAS float* csl = (LAS float*)(smem + L_CS) + wn * 96;
  LAS float* stl = (LAS float*)(smem + L_ST) + wm * 64;
  auto lo2 = [](f32x4 v) { return f32x2{v[0], v[1]}; };
  auto hi2 = [](f32x4 v) { return f32x2{v[2], v[3]}; };
  auto fma2 = [](f32x2 a, f32x2 b, f32x2 c) { return __builtin_elementwise_fma(a, b, c); };
  auto pkv = [](f32x2 v) { return __builtin_bit_cast(u32, __builtin_convertvector(v, bf16x2)); };
  auto phi2 = [&](f32x2 t, f32x2 rs, f32x2 rl, float c1 = 1.f + 1e-6f, float c2 = 1e-6f) {
    const f32x2 u = t * rl, lin = fma2(t, rs, f32x2{c1, c1});
    const f32x2 e = f32x2{__builtin_amdgcn_fmed3f(__builtin_amdgcn_exp2f(u[0]), 0.f, 1.f), __builtin_amdgcn_fmed3f(__builtin_amdgcn_exp2f(u[1]), 0.f, 1.f)} + f32x2{c2, c2};
    return f32x2{fmaxf(lin[0], e[0]), fmaxf(lin[1], e[1])};
  };
  if (TYPE != 1) {
    float smu[4], srs[4];
#pragma unroll
    for (int m = 0; m < 4; m++) { smu[m] = stl[m * 16 + c15]; srs[m] = stl[256 + m * 16 + c15]; }
    f32x4 cv[NT], cw[NT];
#pragma unroll 1
    for (u32 spins = 0; spins < (SPIN_CAP << 4); spins++) {
      const u32 cnt = *(volatile LAS u32*)(smem + L_CNT + 4);
      asm volatile("" ::: "memory");
#pragma unroll
      for (int n = 0; n < NT; n++) { cv[n] = *(LAS f32x4*)(csl + n * 16 + g * 4); cw[n] = *(LAS f32x4*)(csl + 192 + n * 16 + g * 4); }
      asm volatile("" ::: "memory");
      SCHED();
      if (__builtin_amdgcn_readfirstlane(cnt) >= 4) break;
    }
#pragma unroll
    for (int n = 0; n < NT; n++) cv[n] += cw[n];
    u32 odd[4][2];
#pragma unroll
    for (int m = 0; m < 4; m++) {
      const float mu1 = smu[m], r = srs[m];
      const f32x2 mu = {mu1, mu1};
      u32 o[5][2];
      if (TYPE == 0) {
        const f32x2 r2 = {r * r, r * r};
#pragma unroll
        for (int n = 0; n < 3; n++)
#pragma unroll
          for (int h = 0; h < 2; h++) {
            const f32x2 gt = fma2(mu, h ? hi2(cv[n]) : lo2(cv[n]), h ? hi2(acc[m][n]) : lo2(acc[m][n]));
            const f32x2 up = fma2(mu, h ? hi2(cv[(n + 3) % NT]) : lo2(cv[(n + 3) % NT]), h ? hi2(acc[m][(n + 3) % NT]) : lo2(acc[m][(n + 3) % NT]));
            o[n][h] = pkv(f32x2{fmaxf(gt[0], 0.f), fmaxf(gt[1], 0.f)} * (up * r2));
          }
        const int base = WS_HF + ((rb * 4 + wm) * 54 + cb * 3) * 4096;
        ST128((u32x4{o[0][0], o[0][1], o[1][0], o[1][1]}), wsr, l16 + m * 1024, base + wn * 8192);
        odd[m][0] = o[2][0]; odd[m][1] = o[2][1];
        if (m & 1) {
          const auto s0 = __builtin_amdgcn_permlane32_swap(odd[m - 1][0], odd[m][0], false, false);
          const auto s1 = __builtin_amdgcn_permlane32_swap(odd[m - 1][1], odd[m][1], false, false);
          ST128((u32x4{s0[0], s1[0], s0[1], s1[1]}), wsr, (lane >> 5) * 1024 + (lane & 31) * 16 + (m >> 1) * 2048, base + 4096 + wn * 512);
        }
      } else {
        const f32x2 rs = {r, r}, rl = {r * 1.44269504089f, r * 1.44269504089f};
#pragma unroll
        for (int n = 0; n < 5; n++) {
          o[n][0] = pkv(phi2(fma2(mu, lo2(cv[n]), lo2(acc[m][n])), rs, rl));
          o[n][1] = pkv(phi2(fma2(mu, hi2(cv[n]), hi2(acc[m][n])), rs, rl));
        }
        const int base = WS_QF + ((rb * 16 + wm * 4 + m) * 8 + cb * 2 + wn) * 2560;
        ST128((u32x4{o[0][0], o[0][1], o[1][0], o[1][1]}), wsr, l16, base);
        ST128((u32x4{o[2][0], o[2][1], o[3][0], o[3][1]}), wsr, l16 + 1024, base);
        ST64((u32x2{o[4][0], o[4][1]}), wsr, lane * 8 + 2048, base);
      }
    }
  } else {
    f32x4 smu[4], srs[4];
#pragma unroll
    for (int m = 0; m < 4; m++) { smu[m] = *(LAS f32x4*)(stl + m * 16 + g * 4); srs[m] = *(LAS f32x4*)(stl + 256 + m * 16 + g * 4); }
    float ca[5], cb_[5];
#pragma unroll 1
    for (u32 spins = 0; spins < (SPIN_CAP << 4); spins++) {
      const u32 cnt = *(volatile LAS u32*)(smem + L_CNT + 4);
      asm volatile("" ::: "memory");
#pragma unroll
      for (int n = 0; n < 5; n++) { ca[n] = csl[n * 16 + c15]; cb_[n] = csl[192 + n * 16 + c15]; }
      asm volatile("" ::: "memory");
      SCHED();
      if (__builtin_amdgcn_readfirstlane(cnt) >= 4) break;
    }
    f32x2 cv[5];
#pragma unroll
    for (int n = 0; n < 5; n++) { const float v = ca[n] + cb_[n]; cv[n] = f32x2{v, v}; }
    const float mc1 = c15 < 8 ? 1.f + 1e-6f : 0.f, mc2 = c15 < 8 ? 1e-6f : -__builtin_inff();
    f32x4 kv[7], kz[3];
    constexpr int ja[7] = {0, 0, 1, 1, 1, 2, 2}, jb[7] = {2, 3, 2, 3, 4, 3, 4};
#pragma unroll
    for (int mp = 0; mp < 2; mp++) {
      u32x4 fr[5];
#pragma unroll
      for (int mm = 0; mm < 2; mm++) {
        const int m = 2 * mp + mm;
        const f32x4 mu4 = smu[m], rs4 = srs[m];
#pragma unroll
        for (int h = 0; h < 2; h++) {
          const f32x2 mu = h ? hi2(mu4) : lo2(mu4), rs = h ? hi2(rs4) : lo2(rs4);
          const f32x2 rl = rs * f32x2{1.44269504089f, 1.44269504089f};
#pragma unroll
          for (int n = 0; n < 5; n++) {
            const f32x2 t = fma2(mu, cv[n], h ? hi2(acc[m][n]) : lo2(acc[m][n]));
            fr[n][2 * mm + h] = n < 2 ? pkv(phi2(t, rs, rl)) : n > 2 ? pkv(t * rs) : pkv(phi2(t, rs, rl, mc1, mc2));
          }
        }
      }
#pragma unroll
      for (int i = 0; i < 7; i++) kv[i] = MFMA16(fr[ja[i]], fr[jb[i]], (mp == 0 ? f32x4{0, 0, 0, 0} : kv[i]));
#pragma unroll
      for (int i = 0; i < 3; i++) kz[i] = MFMA16(fr[i], ones, (mp == 0 ? f32x4{0, 0, 0, 0} : kz[i]));
    }
    int ob[3], eb[3];
#pragma unroll
    for (int i = 0; i < 3; i++) {
      const int d = 16 * i + 4 * g, hl = d >= 20;
      ob[i] = L_R + 10240 + wm * R_SZ + (2 * wn + hl) * 1680 + (d - 20 * hl) * 4;
      eb[i] = d < 40 ? c15 - 40 - 20 * hl : 1000;
    }
#pragma unroll
    for (int i = 0; i < 7; i++) {
      const u32 e = eb[ja[i]] + 16 * jb[i];
      if (e < 20u) *(LAS f32x4*)(smem + ob[ja[i]] + e * 80) = kv[i];
    }
#pragma unroll
    for (int i = 0; i < 3; i++)
      if (c15 == 0 && eb[i] < 0) *(LAS f32x4*)(smem + ob[i] + 1600) = kz[i];
    WAIT_L0(); BAR();
    if (threadIdx.x < 420) {
      LAS char* q = smem + L_R + 10240 + threadIdx.x * 16;
      const f32x4 t = (*(LAS f32x4*)q + *(LAS f32x4*)(q + R_SZ)) + (*(LAS f32x4*)(q + 2 * R_SZ) + *(LAS f32x4*)(q + 3 * R_SZ));
      ST128(__builtin_bit_cast(u32x4, t), wsr, threadIdx.x * 16, WS_KVP + (cb * 8 + rb) * 6720);
    }
  }
  arrive(smem, L_CNT, 8, ws, TYPE == 1 ? WS_FKV + (cb * 16 + rb) * 8 : WS_FG + (rb * 128 + c) * 8, tag, TYPE == 1 ? 1 : 4, 256);
  lds_wait(smem, L_CNT, 8);
}

DI void spare(LAS char* smem, const u16* x, const u16* wqkv, const u16* wgt, const u16* wu, int rb, int c, int cb2, char* ws, u64& tag) {
  const int lane = threadIdx.x & 63;
  const int wave = __builtin_amdgcn_readfirstlane(threadIdx.x >> 6);
  const int g = lane >> 4, c15 = lane & 15;
  const rsrc_t wsr = __builtin_amdgcn_make_buffer_rsrc(ws, 0, WS_END, 0x00020000);
  const int gw = ((c - 30) * 8 + rb) * 8 + wave, mat = gw < 45 ? 0 : gw < 86 ? 1 : 2;
  const int pitch = mat ? F_ * 2 : 3840;
  const u32x4 sr = mk_srd(mat == 0 ? wqkv : mat == 1 ? wgt : wu, D_ * pitch);
  auto wtouch = [&](int r0, int n) {
    const u32 v = (gw - (mat == 0 ? 0 : mat == 1 ? 45 : 86)) * 8192 + lane * 128 + r0 * pitch;
    asm volatile("s_nop 4" ::: "memory");
#pragma unroll
    for (int j = 0; j < n; j++) TOUCH(LDS0 + L_DUMMY, v + j * 96 * pitch, sr, 0);
  };
  {
    const u32x4 sq = mk_srd(wqkv, D_ * 3840);
    asm volatile("s_nop 4" ::: "memory");
    TOUCH(LDS0 + L_DUMMY, (u32)(160 * 3840 + gw * 8192 + lane * 128), sq, 0);
  }
  u32x4 xv[4][5];
  const int row0 = rb * 256 + (c - 30) * 128 + wave * 16;
#pragma unroll
  for (int p = 0; p < 4; p++)
#pragma unroll
    for (int i = 0; i < 5; i++) xv[p][i] = *(const u32x4*)(x + (row0 + 4 * p + g) * D_ + (c15 + 16 * i) * 8);
  SCHED();
  tag = get_tag(ws);
  auto row_sum = [](float v) {
    v += __builtin_bit_cast(float, __builtin_amdgcn_mov_dpp(__builtin_bit_cast(int, v), 0xB1, 0xF, 0xF, true));
    v += __builtin_bit_cast(float, __builtin_amdgcn_mov_dpp(__builtin_bit_cast(int, v), 0x4E, 0xF, 0xF, true));
    v += __builtin_bit_cast(float, __builtin_amdgcn_mov_dpp(__builtin_bit_cast(int, v), 0x141, 0xF, 0xF, true));
    v += __builtin_bit_cast(float, __builtin_amdgcn_mov_dpp(__builtin_bit_cast(int, v), 0x140, 0xF, 0xF, true));
    return v;
  };
#pragma unroll
  for (int p = 0; p < 4; p++) {
    float f[40], s0 = 0, s1 = 0;
#pragma unroll
    for (int i = 0; i < 20; i++) { f[2 * i] = blo(xv[p][i >> 2][i & 3]); f[2 * i + 1] = bhi(xv[p][i >> 2][i & 3]); s0 += f[2 * i]; s1 += f[2 * i + 1]; }
    const float m = row_sum(s0 + s1) * (1.f / D_);
    float q0 = 0, q1 = 0;
#pragma unroll
    for (int i = 0; i < 20; i++) { const float a = f[2 * i] - m, b = f[2 * i + 1] - m; q0 = __builtin_fmaf(a, a, q0); q1 = __builtin_fmaf(b, b, q1); }
    const float q = row_sum(q0 + q1);
    const float sd = rbf(sqrtf(rbf(rbf(q * (1.f / D_)) + 1e-5f)));
    __builtin_amdgcn_raw_buffer_store_b32(__float_as_uint(c15 ? 1.f / sd : -rbf(m)), wsr,
                                          c15 < 2 ? WS_STAT + rb * 2048 + c15 * 1024 + ((row0 & 255) + 4 * p + g) * 4 : OOB, 0, SC1);
    SCHED();
  }
  WAIT_V(0); WAIT_L0(); BAR();
  if (threadIdx.x == 0) flag_st(ws, WS_FP + (c * 8 + rb) * 8, tag);
  wtouch(352, 3);
  if (wave < 4) {
    const u32 fso = WS_FP + (wave * 64 + lane) * 8;
    flag_wait<48>(ws, fso, tag, ~tag);
  }
  BAR();
  if (c == 31 && rb == 0 && threadIdx.x == 0) flag_st(ws, WS_EPOCH, tag);
  if (wave < 4) {
    const u32x4 srdp = mk_srd(ws, WS_END);
    const u32 lds0 = LDS0, l16 = lane * 16;
    const u32 psrc = WS_WDF + (cb2 * 54 + d_k0(wave) + d_step(4, d_rot())) * 5120;
    asm volatile("s_nop 4" ::: "memory");
#pragma unroll
    for (int i = 0; i < 20; i++) DMA16C(lds0 + p_off(i) + p_base(wave, i), l16, srdp, psrc + i * 1024);
  }
}

DI void phase2(LAS char* smem, const u16* x, u16* out, int rb, int cb, int red, char* ws, u64 tag) {
  const int lane = threadIdx.x & 63;
  const int wave = __builtin_amdgcn_readfirstlane(threadIdx.x >> 6);
  const int w = wave & 3;
  const u32 l16 = lane * 16;
  const u32 lds0 = LDS0;
  const rsrc_t wsr = __builtin_amdgcn_make_buffer_rsrc(ws, 0, WS_END, 0x00020000);
  const f32x4 z4 = {0, 0, 0, 0};
  auto tile = [&](int i) { return (w + 1 + i) & 3; };
  u32x2 xr[3];
  u32 xo;
  auto ldx = [&](int n0, int n1) {
    u32 l = l16;
    asm volatile("" : "+v"(l));
    xo = (rb * 64 + w * 16 + ((l >> 4) & 15)) * D_ + cb * 80 + (l >> 8) * 4;
#pragma unroll
    for (int n = n0; n < n1; n++) xr[n - n0] = *(const u32x2*)(x + xo + n * 16);
  };
  auto dpart = [&](int k, int n) { const int f = (3 - k) * 5 + n; return *(LAS f32x4*)(smem + p_off(f) + p_base((w + k) & 3, f) + l16); };
  auto spart = [&](int k, int n) { return *(LAS f32x4*)(smem + L_R + ((w + k) & 3) * R_SZ + ((3 - k) * 5 + n - (k == 0 ? 3 : 0)) * 1024 + l16); };
  auto finish = [&](f32x4 bs, f32x4 ss, u32x2 xv, int n) {
    float r[4];
#pragma unroll
    for (int j = 0; j < 4; j++) {
      const float v = bs[j], gt = v * __builtin_amdgcn_rcpf(1.f + __builtin_amdgcn_exp2f(-1.44269504089f * v));
      r[j] = ((j & 1) ? bhi(xv[j >> 1]) : blo(xv[j >> 1])) + ss[j] * gt;
    }
    *(u32x2*)(out + xo + n * 16) = u32x2{pk2(r[0], r[1]), pk2(r[2], r[3])};
  };
  if (wave < 4) {
    const int k0 = d_k0(w);
    const int ha = WS_HF + (rb * 54 + k0) * 4096, wa = WS_WDF + (cb * 54 + k0) * 5120;
    const int rot = d_rot();
    auto vof = [&](int s) { return (s == 3 || s == 8 || s == 13) ? l16 + ((w >= 2 && s + rot == 13) ? OOB : 0) : l16; };
    u32x4 a[14][4], b[14][5];
    auto lda = [&](int s) {
#pragma unroll
      for (int t = 0; t < 4; t++) a[s][t] = LD128(wsr, vof(s), ha + d_step(s, rot) * 4096 + tile(t) * 1024, SC1);
    };
    auto ldb = [&](int s) {
#pragma unroll
      for (int n = 0; n < 5; n++) b[s][n] = LD128(wsr, vof(s) + (n & 3) * 1024, wa + d_step(s, rot) * 5120 + (n >> 2) * 4096, SC1);
    };
    auto ldl = [&](int s) {
#pragma unroll
      for (int n = 0; n < 5; n++) { const int f = (s - 4) * 5 + n; b[s][n] = *(LAS u32x4*)(smem + p_off(f) + p_base(w, f) + l16); }
    };
    __builtin_amdgcn_s_setprio(3);
#pragma unroll
    for (int s = 0; s < 4; s++) { ldb(s); SCHED(); }
    if (red >= 0) {
      const int hg = red >> 1, q = (red & 1) * 216 + threadIdx.x;
      group_wait<8, 2>(smem, L_CNT + 32, w == 0, ws, WS_FKV + hg * 128, tag);
      f32x4 kp[8];
#pragma unroll
      for (int p = 0; p < 8; p++)
        kp[p] = __builtin_bit_cast(f32x4, LD128(wsr, threadIdx.x < 216 && q < 420 ? q * 16 : OOB, WS_KVP + (hg * 8 + p) * 6720, SC1));
      const f32x4 t = ((kp[0] + kp[1]) + (kp[2] + kp[3])) + ((kp[4] + kp[5]) + (kp[6] + kp[7]));
      ST64((u32x2{pk2(t[0], t[1]), pk2(t[2], t[3])}), wsr, threadIdx.x < 216 ? q * 8 : OOB, WS_KVC + hg * 3456);
      arrive(smem, L_CNT + 8, 4, ws, WS_FK + red * 8, tag, 32, 128);
      lds_wait(smem, L_CNT + 8, 4);
    }
    group_wait<18, 2>(smem, L_CNT + 36, w == 0 && red < 0, ws, WS_FG + rb * 256, tag);
    SCHED();
#pragma unroll
    for (int s = 0; s < 5; s++) { lda(s); SCHED(); }
    __builtin_amdgcn_s_setprio(0);
    f32x4 acc[4][5];
#pragma unroll
    for (int s = 0; s < 11; s++) {
      SCHED();
#pragma unroll
      for (int t = 0; t < 4; t++)
#pragma unroll
        for (int n = 0; n < 5; n++) acc[t][n] = MFMA16(b[s][n], a[s][t], (s == 0 ? z4 : acc[t][n]));
      if (s == 1) { lda(5); lda(6); }
      if (s == 2) { lda(7); }
      if (s == 3) { lda(8); }
      if (s == 4) { ldb(8); lda(9); }
      if (s == 5) { ldb(9); }
      if (s == 6) { lda(10); }
      if (s == 7) { ldb(10); }
      if (s == 8) { lda(11); ldb(11); }
      if (s == 9) { lda(12); ldb(12); }
      if (s == 10) { lda(13); ldb(13); }
      if (s == 2) ldl(4);
      if (s >= 4 && s <= 6) ldl(s + 1);
      if (s == 10) ldx(3, 5);
#pragma unroll
      for (int i = 0; i < 10; i++) {
        __builtin_amdgcn_sched_group_barrier(0x8, 2, 2);
        __builtin_amdgcn_sched_group_barrier(0x20, 1, 2);
        __builtin_amdgcn_sched_group_barrier(0x100, 1, 2);
      }
      SCHED();
    }
    {
      auto tail = [&](int t, int n0, int n1) {
#pragma unroll
        for (int s = 11; s < 14; s++)
#pragma unroll
          for (int n = n0; n < n1; n++) acc[t][n] = MFMA16(b[s][n], a[s][t], acc[t][n]);
      };
      auto dump = [&](int t, int n1) {
#pragma unroll
        for (int n = 0; n < n1; n++) *(LAS f32x4*)(smem + p_off(t * 5 + n) + p_base(w, t * 5 + n) + l16) = acc[t][n];
      };
      auto weave = [&]() {
#pragma unroll
        for (int i = 0; i < 5; i++) { __builtin_amdgcn_sched_group_barrier(0x8, 2, 5); __builtin_amdgcn_sched_group_barrier(0x200, 1, 5); }
      };
      tail(0, 0, 5); SCHED();
      tail(1, 0, 5); dump(0, 5); weave(); SCHED();
      tail(2, 0, 5); dump(1, 5); weave(); SCHED();
      tail(3, 0, 3); dump(2, 5); weave(); SCHED();
      tail(3, 3, 5); dump(3, 3); weave(); SCHED();
    }
    WAIT_L0(); BAR();
#pragma unroll
    for (int n = 3; n < 5; n++) {
      const f32x4 bs = (acc[3][n] + dpart(1, n)) + (dpart(2, n) + dpart(3, n));
      const f32x4 ss = (spart(0, n) + spart(1, n)) + (spart(2, n) + spart(3, n));
      finish(bs, ss, xr[n - 3], n);
    }
  } else {
    const int g = lane >> 4, c15 = lane & 15;
    const u32x4 srdp = mk_srd(ws, WS_END);
    const u32 rl = lds0 + L_R + w * R_SZ;
    __builtin_amdgcn_s_setprio(2);
    u32x4 wf[5][5], pq[4][2][2];
    u32x4 ph[4];
    const int wa = WS_WOF + (cb * 20 + w * 5) * 5120;
#pragma unroll
    for (int s = 0; s < 3; s++)
#pragma unroll
      for (int n = 0; n < 5; n++) wf[s][n] = LD128(wsr, l16 + (n & 3) * 1024, wa + s * 5120 + (n >> 2) * 4096, SC1);
    asm volatile("s_nop 4" ::: "memory");
    if (red >= 0) {
#pragma unroll
      for (int i = 0; i < 10; i++) DMA16C(rl + i * 1024, l16, srdp, wa + 3 * 5120 + i * 1024);
    }
    SCHED();
    constexpr int ga[14][2] = {{0, 0}, {0, 1}, {1, 1}, {1, 2}, {2, 2}, {2, 3}, {3, 3}, {3, 4}, {4, 0}, {4, 1}, {4, 2}, {5, 2}, {5, 3}, {5, 4}};
    u32 gad[14];
#pragma unroll
    for (int k = 0; k < 14; k++) {
      const int j = ga[k][0], hd = j < 4 ? j : 2 * (j - 4) + (c15 & 1), row = j < 4 ? c15 : (c15 & 2) ? 20 : 16 + (c15 >> 2);
      const u32 d = 16 * ga[k][1] + 4 * g - 20 * hd;
      gad[k] = rl + 10240 + (d < 20u ? hd * 840 + row * 40 + d * 2 : 3360);
      asm volatile("" : "+v"(gad[k]));
    }
    group_wait<4>(smem, L_CNT + 44, w == 0, ws, WS_FG + rb * 256 + 208, tag);
#pragma unroll
    for (int h = 0; h < 2; h++)
#pragma unroll
      for (int t = 0; t < 4; t++) {
        const int qa = WS_QF + ((rb * 4 + tile(t)) * 8 + 2 * w + h) * 2560;
        pq[t][h][0] = LD128(wsr, l16, qa, SC1); pq[t][h][1] = LD128(wsr, l16 + 1024, qa, SC1);
        const u32x2 v = LD64(wsr, lane * 8 + 2048, qa, SC1);
        ph[t][2 * h] = v[0]; ph[t][2 * h + 1] = v[1];
      }
    SCHED();
    if (red >= 0 && w == 0) group_wait<18>(smem, L_CNT + 36, true, ws, WS_FG + rb * 256, tag);
    group_wait<16>(smem, L_CNT + 40, w == 0, ws, WS_FK + rb * 128, tag);
#pragma unroll
    for (int i = 0; i < 7; i++) DMA16C(rl + 10240 + i * 1024, l16, srdp, WS_KVC + w * 6912 + i * 1024);
    WAIT_V(0);
    SCHED();
    {
      u32x2 gq[2][14];
      f32x4 on[2][6];
      const f32x4 e4 = {0.f, 0.f, 1e-6f, 1e-6f};
      auto mm = [&](int u) {
        const int h = u >> 2, t = u & 3;
        auto fr2 = [&](int lo, int hi) { return u32x4{lo < 0 ? 0u : gq[h][lo][0], lo < 0 ? 0u : gq[h][lo][1], hi < 0 ? 0u : gq[h][hi][0], hi < 0 ? 0u : gq[h][hi][1]}; };
        auto fr5 = [&](int k) { return h ? fr2(-1, k) : fr2(k, -1); };
        f32x4(&o)[6] = on[u & 1];
        const u32x4 p0 = pq[t][h][0], p1 = pq[t][h][1], p2 = ph[t];
        o[4] = MFMA16(fr2(8, 9), p0, e4);
        o[5] = MFMA16(fr2(11, 12), p1, e4);
        o[1] = MFMA16(fr2(-1, 2), p0, z4);
        o[3] = MFMA16(fr2(-1, 6), p1, z4);
        o[4] = MFMA16(fr2(10, -1), p1, o[4]);
        o[5] = MFMA16(fr5(13), p2, o[5]);
        o[0] = MFMA16(fr2(0, 1), p0, z4);
        o[2] = MFMA16(fr2(4, 5), p1, z4);
        o[1] = MFMA16(fr2(3, -1), p1, o[1]);
        o[3] = MFMA16(fr5(7), p2, o[3]);
      };
      auto scale = [&](int u) {
        const int h = u >> 2, t = u & 3;
        f32x4(&o)[6] = on[u & 1];
        float rd[4];
#pragma unroll
        for (int j = 0; j < 4; j++) rd[j] = __builtin_amdgcn_rcpf(o[4 + (j >> 1)][2 + (j & 1)]);
        u32 op[4][2];
#pragma unroll
        for (int j = 0; j < 4; j++) { op[j][0] = pkm(o[j][0], rd[j], o[j][1], rd[j]); op[j][1] = pkm(o[j][2], rd[j], o[j][3], rd[j]); }
        pq[t][h][0] = u32x4{op[0][0], op[0][1], op[1][0], op[1][1]};
        pq[t][h][1] = u32x4{op[2][0], op[2][1], op[3][0], op[3][1]};
        ph[t][2 * h] = pkm(o[4][0], rd[0], o[4][1], rd[1]); ph[t][2 * h + 1] = pkm(o[5][0], rd[2], o[5][1], rd[3]);
      };
      auto gather = [&](int h) {
#pragma unroll
        for (int k = 0; k < 14; k++) gq[h][k] = *(LAS u32x2*)((LAS char*)(size_t)gad[k] + h * 3456);
      };
      gather(0); gather(1);
#pragma unroll
      for (int u = 0; u < 9; u++) {
        if (u == 5) { ldx(0, 3); SCHED(); }
        if (u == 8) {
#pragma unroll
          for (int i = 0; i < 10; i++) wf[3 + i / 5][i % 5] = *(LAS u32x4*)((LAS char*)(size_t)rl + i * 1024 + l16);
          SCHED();
        }
        if (u < 8) mm(u);
        if (u > 0) scale(u - 1);
#pragma unroll
        for (int i = 0; i < 10; i++) { __builtin_amdgcn_sched_group_barrier(0x8, 1, 1); __builtin_amdgcn_sched_group_barrier(0x2, 4, 1); }
        SCHED();
      }
    }
    WAIT_L0();
    SCHED();
    f32x4 ac2[2][5];
    auto sg = [&](int t, int n0, int n1) {
#pragma unroll
      for (int s = 0; s < 5; s++) {
        const u32x4 o = s < 4 ? pq[t][s >> 1][s & 1] : ph[t];
#pragma unroll
        for (int n = n0; n < n1; n++) ac2[t & 1][n] = MFMA16(wf[s][n], o, (s == 0 ? z4 : ac2[t & 1][n]));
      }
    };
    auto dump = [&](int t, int n0) {
#pragma unroll
      for (int n = n0; n < 5; n++) *(LAS f32x4*)((LAS char*)(size_t)rl + (t * 5 + n - n0) * 1024 + l16) = ac2[t & 1][n];
    };
    auto weave = [&]() {
#pragma unroll
      for (int i = 0; i < 5; i++) { __builtin_amdgcn_sched_group_barrier(0x8, 2, 3); __builtin_amdgcn_sched_group_barrier(0x200, 1, 3); }
    };
    sg(3, 3, 5); SCHED();
    sg(0, 0, 5); dump(3, 3); weave(); SCHED();
    sg(1, 0, 5); dump(0, 0); weave(); SCHED();
    sg(2, 0, 5); dump(1, 0); weave(); SCHED();
    sg(3, 0, 3); dump(2, 0); weave(); SCHED();
    f32x4(&acc)[5] = ac2[1];
    WAIT_L0(); BAR();
#pragma unroll
    for (int n = 0; n < 3; n++) {
      const f32x4 bs = (dpart(0, n) + dpart(1, n)) + (dpart(2, n) + dpart(3, n));
      const f32x4 ss = (acc[n] + spart(1, n)) + (spart(2, n) + spart(3, n));
      finish(bs, ss, xr[n], n);
    }
  }
}

__global__ __launch_bounds__(512, 2) void fused(const u16* __restrict__ x, const u16* __restrict__ wqkv, const u16* __restrict__ wo,
                                                const u16* __restrict__ wgt, const u16* __restrict__ wu, const u16* __restrict__ wd,
                                                u16* __restrict__ out, char* ws) {
  extern __shared__ __attribute__((aligned(16))) char smem_raw[];
  LAS char* smem = (LAS char*)smem_raw;
  const int xcd = blockIdx.x & 7, idx = blockIdx.x >> 3;
  const int rb = (xcd >> 2) * 4 + (idx < 28 ? (idx & 3) : (xcd & 2) + (idx & 1));
  const int c = idx < 28 ? (idx >> 2) * 4 + (xcd & 3) : 28 + (idx & 2) + (xcd & 1);
  const int rb2 = xcd * 4 + (idx >> 3), cb2 = idx & 7;
  u64 tag;
  if (threadIdx.x < 16) ((LAS u32*)(smem + L_CNT))[threadIdx.x] = 0;
  u64 pc;
  asm volatile("s_getpc_b64 %0" : "=s"(pc));
  if (__builtin_amdgcn_readfirstlane(threadIdx.x >> 6) == 7) {
    const u32x4 srdc = mk_srd((const void*)pc, CODE_BYTES);
    asm volatile("s_nop 4" ::: "memory");
    TOUCH(LDS0 + L_DUMMY, (u32)((idx & 7) * 8192 + (threadIdx.x & 63) * 128), srdc, 0);
  }
  if (c >= 30) spare(smem, x, wqkv, wgt, wu, rb, c, cb2, ws, tag);
  else {
    const int lane = threadIdx.x & 63;
    const int wave = __builtin_amdgcn_readfirstlane(threadIdx.x >> 6);
    const int grp = wave >> 2, wg = wave & 3, img = wg >> 1;
    const u32 lds0 = LDS0;
    P1 p;
    const u32x4 srdx = mk_srd(x + (size_t)rb * 256 * D_, 256 * D_ * 2);
    u32 vx[2];
#pragma unroll
    for (int i = 0; i < 2; i++) { const int r = 8 * i + (lane >> 3); vx[i] = (64 * wg + r) * (D_ * 2) + (((lane & 7) ^ ((r >> 1) & 7)) << 4); }
    const int unit = ((c < 18 ? c : c >= 26 ? c - 8 : c + 4) * 8 + rb) * 4 + wg,
              uu = unit < 740 ? unit : 739, uks = uu / 10, ucg = uu - uks * 10;
    if (grp) BAR();
    asm volatile("s_nop 4" ::: "memory");
#pragma unroll
    for (int i = 0; i < 8; i++) DMA16C(lds0 + grp * L_X1 + wg * 8192 + i * 1024, vx[i & 1], srdx, grp * 128 + (i >> 1) * 16 * D_ * 2);
    int col;
    if (c < 18) col = c * 96;
    else if (c < 26) col = 640 + img * 640 + 80 * (c - 18) - 16 * (c & 1);
    else col = 160 * (c - 26) + img * 64;
    p.pitch = c < 18 ? F_ * 2 : 3840;
    const u16* wb = (c < 18 ? (img ? wu : wgt) : wqkv) + col;
    const u32x4 srdw = mk_srd(wb, D_ * p.pitch - col * 2);
#pragma unroll
    for (int i = 0; i < 6; i++) { const int o = (6 * (wg & 1) + i) * 1024 + lane * 16, r = o / 192; p.vo[i] = r * p.pitch + ((o - r * 192) ^ ((r & 8) << 2)); }
    asm volatile("s_nop 4" ::: "memory");
#pragma unroll
    for (int i = 0; i < 6; i++) DMA16C(lds0 + (grp ? L_W1 : L_W0) + wg * 6144 + i * 1024, p.vo[i], srdw, grp * 64 * p.pitch);
    if (!grp) { WAIT_L0(); BAR(); }
    if (grp) {
      int r0, r1;
      bool rem = false;
      if (uks < 54) { r0 = 32 * uks; r1 = r0 + 16; }
      else {
        const int k = uks - 54, pr = k / 5, s5 = k - pr * 5;
        if (s5 < 4) { r0 = 160 * pr + 40 * s5; r1 = r0 + 20; } else { r0 = 160 * pr + 16; r1 = r0 + 80; rem = true; }
      }
      const u32x4 srdu = uks < 54 ? mk_srd(wd, F_ * D_ * 2) : mk_srd(wo, unit < 740 ? D_ * D_ * 2 : 0);
      const u32 vu = (rem ? 20 * ((lane >> 3) & 3) + (lane >> 5) : lane >> 3) * (D_ * 2) + (lane & 7) * 16;
      asm volatile("s_nop 4" ::: "memory");
#pragma unroll
      for (int i = 0; i < 4; i++) DMA16NT(lds0 + L_SP + wg * 4096 + i * 1024, vu, srdu, ((i < 2 ? r0 : r1) + (rem ? 2 : 8) * (i & 1)) * (D_ * 2) + ucg * 128);
    }
    {
      const int g = lane >> 4;
      const bool alt = uks < 54 && uks % 3 == 1;
      p.ua = lds0 + L_SP + wg * 4096 + ((alt ? 16 * (g >> 1) + 4 * (g & 1) : 4 * g) + ((lane & 15) >> 2)) * 128 + (lane & 3) * 8;
      p.ub = p.ua + (alt ? 8 * 128 : 16 * 128);
#pragma unroll
      for (int j = 0; j < 4; j++) {
        const int n16 = ucg * 4 + j, cbk = n16 / 5, nt = n16 - cbk * 5;
        p.ud[j] = unit >= 740 ? OOB : (uks < 54 ? WS_WDF + (cbk * 54 + uks) * 5120 : WS_WOF + (cbk * 20 + uks - 54) * 5120) + nt * 1024;
      }
    }
    tag = get_tag(ws);
    if (grp == 0) {
      p.srd = srdx; p.vo[0] = vx[0]; p.vo[1] = vx[1];
    } else {
      p.srd = srdw;
    }
    if (c < 18) phase1<0>(smem, p, 0, 0, rb, c, c, cb2, ws, tag);
    else if (c < 26) phase1<1>(smem, p, c & 1, c & 1, rb, c - 18, c, cb2, ws, tag);
    else phase1<2>(smem, p, 0, 1, rb, c - 26, c, cb2, ws, tag);
  }
  phase2(smem, x, out, rb2, cb2, c >= 30 ? rb * 2 + c - 30 : -1, ws, tag);
}

extern "C" void kernel_launch(void* const* d_in, const int* in_sizes, int n_in, void* d_out, int out_size, void* d_ws, size_t ws_size,
                              hipStream_t stream) {
  (void)hipFuncSetAttribute((const void*)fused, hipFuncAttributeMaxDynamicSharedMemorySize, LDS_BYTES);
  fused<<<256, 512, LDS_BYTES, stream>>>((const u16*)d_in[0], (const u16*)d_in[1], (const u16*)d_in[2], (const u16*)d_in[4],
                                         (const u16*)d_in[5], (const u16*)d_in[6], (u16*)d_out, (char*)d_ws);
}
```
